```python
import torch

def _softmax(x, axis=-1):
    return torch.softmax(x, dim=axis)
def _gelu(x):
    return torch.nn.functional.gelu(x, approximate="tanh")
def _silu(x):
    return torch.nn.functional.silu(x)
def _sigmoid(x):
    return torch.sigmoid(x)
def _layernorm(x, axis=-1, eps=1e-5):
    m = x.mean(dim=axis, keepdim=True)
    v = x.var(dim=axis, keepdim=True, unbiased=False)
    return (x - m) / torch.sqrt(v + eps)
def _scatter_add(tgt, idx, src, axis=0):
    return tgt.clone().index_add_(axis, idx, src)
def _segment_sum(src, seg, n):
    out = torch.zeros((n,) + src.shape[1:], dtype=src.dtype)
    return out.index_add_(0, seg, src)
def _pad(x, pw):
    flat = tuple(v for pair in reversed(pw) for v in pair)
    return torch.nn.functional.pad(x, flat)

kernel_name = 'parallel_sum__linear_kernel_hedgehog__glu_gated_geglu__prompt'

def setup_inputs():

    _w = (lambda a: (lambda t: t.to(torch.bfloat16) if t.is_floating_point() else t)(torch.as_tensor(a)))
    def _A_setup():
        B, S, D, H = 1, 2048, 640, 32
        Dh = D // H
        return {
            "x": _w((lambda *s: torch.randn(*s, dtype=torch.bfloat16) * 0.05)(B, S, D)),
            "wqkv": _w((lambda *s: torch.randn(*s, dtype=torch.bfloat16) * 0.2)(D, 3 * D)),
            "w_phi": _w((lambda *s: torch.randn(*s, dtype=torch.bfloat16) * 0.2)(H, Dh, Dh) * (Dh ** -0.5)),
            "wo": _w((lambda *s: torch.randn(*s, dtype=torch.bfloat16) * 0.05)(D, D)),
        }
    _A_in = _A_setup()

    def _B_setup():
        B, S, Dm, Df = 1, 2048, 640, 1536
        return {
            "x": _w((lambda *s: torch.randn(*s, dtype=torch.bfloat16) * 0.05)(B, S, Dm)),
            "w_gate": _w((lambda *s: torch.randn(*s, dtype=torch.bfloat16) * 0.4)(Dm, Df) * (Dm ** -0.5)),
            "w_up": _w((lambda *s: torch.randn(*s, dtype=torch.bfloat16) * 0.4)(Dm, Df) * (Dm ** -0.5)),
            "w_down": _w((lambda *s: torch.randn(*s, dtype=torch.bfloat16) * 0.4)(Df, Dm) * (Df ** -0.5)),
        }
    _B_in = _B_setup()

    return {
        "A_x": _A_in["x"],
        "A_wqkv": _A_in["wqkv"],
        "A_w_phi": _A_in["w_phi"],
        "A_wo": _A_in["wo"],
        "B_x": _B_in["x"],
        "B_w_gate": _B_in["w_gate"],
        "B_w_up": _B_in["w_up"],
        "B_w_down": _B_in["w_down"],
    }

def reference(A_x, A_wqkv, A_w_phi, A_wo, B_x, B_w_gate, B_w_up, B_w_down):
    def _A_ref(x, wqkv, w_phi, wo):
        B, S, D = x.shape
        H = 32; Dh = D // H
        qkv = (lambda x, *d: x.permute(*d))((x @ wqkv).reshape(B, S, 3, H, Dh), 2, 0, 3, 1, 4)
        q, k, v = qkv[0], qkv[1], qkv[2]
        pq = torch.einsum("bhsd,hdf->bhsf", q, w_phi)
        pk = torch.einsum("bhsd,hdf->bhsf", k, w_phi)
        phi_q = torch.concatenate([_softmax(pq, axis=-1), _softmax(-pq, axis=-1)], axis=-1)
        phi_k = torch.concatenate([_softmax(pk, axis=-1), _softmax(-pk, axis=-1)], axis=-1)
        kv = torch.einsum("bhsf,bhsd->bhfd", phi_k, v)
        z  = torch.einsum("bhsf->bhf", phi_k)
        num = torch.einsum("bhqf,bhfd->bhqd", phi_q, kv)
        den = torch.einsum("bhqf,bhf->bhq", phi_q, z)[..., None] + 1e-6
        o = num / den
        return (lambda x, *d: x.permute(*d))(o, 0, 2, 1, 3).reshape(B, S, D) @ wo

    def _B_ref(x, w_gate, w_up, w_down):
        g = torch.einsum("bsd,df->bsf", x, w_gate)
        u = torch.einsum("bsd,df->bsf", x, w_up)
        h = _gelu(g) * u
        return torch.einsum("bsf,fd->bsd", h, w_down)

    x = A_x
    xn = _layernorm(x, axis=-1)
    return x + _A_ref(xn, A_wqkv, A_w_phi, A_wo) + _B_ref(xn, B_w_gate, B_w_up, B_w_down)
```

```cpp
#include <hip/hip_runtime.h>
#include <hip/hip_bf16.h>
typedef __hip_bfloat16 bf16;
__global__ void k_ln(const bf16* x, float* xn){
  int row = blockIdx.x; int t = threadIdx.x;
  __shared__ float red[64];
  float s=0; for(int i=t;i<640;i+=64) s += (float)x[row*640+i];
  for(int o=32;o;o>>=1) s += __shfl_xor(s,o);
  float m = s/640.f; float v=0;
  for(int i=t;i<640;i+=64){ float d=(float)x[row*640+i]-m; v+=d*d;}
  for(int o=32;o;o>>=1) v += __shfl_xor(v,o);
  float r = 1.f/sqrtf(v/640.f+1e-5f);
  for(int i=t;i<640;i+=64) xn[row*640+i] = ((float)x[row*640+i]-m)*r;
}
__global__ void k_gemm(const float* A, const bf16* W, float* C, int M,int N,int K, int ldc){
  int n = blockIdx.x*64+threadIdx.x; int m0 = blockIdx.y*8;
  if(n>=N) return;
  float acc[8]={0,0,0,0,0,0,0,0};
  for(int k=0;k<K;k++){ float w=(float)W[k*N+n];
    #pragma unroll
    for(int i=0;i<8;i++) acc[i]+=A[(m0+i)*K+k]*w; }
  for(int i=0;i<8;i++) C[(m0+i)*ldc+n]=acc[i];
}
__global__ void k_phi(const float* qkv, const bf16* wphi, float* phiq, float* phik){
  int s = blockIdx.x; int t = threadIdx.x;
  int h = t&31, which = t>>5;
  float q[20], p[20];
  for(int d=0;d<20;d++) q[d]=qkv[s*1920+which*640+h*20+d];
  for(int f=0;f<20;f++){ float a=0; for(int d=0;d<20;d++) a+=q[d]*(float)wphi[h*400+d*20+f]; p[f]=a; }
  float mx=-1e30f,mn=1e30f; for(int f=0;f<20;f++){mx=fmaxf(mx,p[f]);mn=fminf(mn,p[f]);}
  float s1=0,s2=0; for(int f=0;f<20;f++){ s1+=expf(p[f]-mx); s2+=expf(mn-p[f]); }
  float* o = (which? phik:phiq) + (s*32+h)*40;
  for(int f=0;f<20;f++){ o[f]=expf(p[f]-mx)/s1; o[20+f]=expf(mn-p[f])/s2; }
}
__global__ void k_kv(const float* phik, const float* qkv, float* kv){
  int h = blockIdx.x; int t = threadIdx.x; if(t>=840) return;
  int f=t/21,d=t%21; float a=0;
  for(int s=0;s<2048;s++){ float v = d<20? qkv[s*1920+1280+h*20+d]:1.f; a+=phik[(s*32+h)*40+f]*v; }
  kv[h*840+t]=a;
}
__global__ void k_o(const float* phiq, const float* kv, float* o){
  int s=blockIdx.x; int t=threadIdx.x; if(t>=640) return; int h=t/20,d=t%20;
  float num=0,den=0; for(int f=0;f<40;f++){ float p=phiq[(s*32+h)*40+f]; num+=p*kv[h*840+f*21+d]; den+=p*kv[h*840+f*21+20]; }
  o[s*640+t]=num/(den+1e-6f);
}
__global__ void k_h(const float* g,const float* u,float* h,int n){ int i=blockIdx.x*256+threadIdx.x; if(i>=n) return;
  float x=g[i]; float t=tanhf(0.7978845608028654f*(x+0.044715f*x*x*x)); h[i]=0.5f*x*(1.f+t)*u[i]; }
__global__ void k_fin(const bf16* x,const float* a,const float* b,bf16* out,int n){ int i=blockIdx.x*256+threadIdx.x; if(i>=n) return; out[i]=(bf16)((float)x[i]+a[i]+b[i]); }

extern "C" void kernel_launch(void* const* d_in, const int* in_sizes, int n_in,
                              void* d_out, int out_size, void* d_ws, size_t ws_size,
                              hipStream_t stream) {
  const bf16* x=(const bf16*)d_in[0]; const bf16* wqkv=(const bf16*)d_in[1]; const bf16* wphi=(const bf16*)d_in[2]; const bf16* wo=(const bf16*)d_in[3];
  const bf16* wg=(const bf16*)d_in[5]; const bf16* wu=(const bf16*)d_in[6]; const bf16* wd=(const bf16*)d_in[7];
  float* ws=(float*)d_ws; size_t o=0;
  float* xn=ws+o; o+=2048*640; float* qkv=ws+o; o+=2048*1920; float* g=ws+o; o+=2048*1536; float* u=ws+o; o+=2048*1536; float* h=ws+o; o+=2048*1536;
  float* phiq=ws+o; o+=2048*1280; float* phik=ws+o; o+=2048*1280; float* kv=ws+o; o+=32*840; float* oo=ws+o; o+=2048*640; float* A=ws+o; o+=2048*640; float* B=ws+o; o+=2048*640;
  k_ln<<<2048,64,0,stream>>>(x,xn);
  k_gemm<<<dim3(30,256),64,0,stream>>>(xn,wqkv,qkv,2048,1920,640,1920);
  k_gemm<<<dim3(24,256),64,0,stream>>>(xn,wg,g,2048,1536,640,1536);
  k_gemm<<<dim3(24,256),64,0,stream>>>(xn,wu,u,2048,1536,640,1536);
  k_phi<<<2048,64,0,stream>>>(qkv,wphi,phiq,phik);
  k_kv<<<32,1024,0,stream>>>(phik,qkv,kv);
  k_o<<<2048,640,0,stream>>>(phiq,kv,oo);
  k_h<<<2048*1536/256,256,0,stream>>>(g,u,h,2048*1536);
  k_gemm<<<dim3(10,256),64,0,stream>>>(oo,wo,A,2048,640,640,640);
  k_gemm<<<dim3(10,256),64,0,stream>>>(h,wd,B,2048,640,1536,640);
  k_fin<<<2048*640/256,256,0,stream>>>(x,A,B,(bf16*)d_out,2048*640);
}
```

```cpp
#include <hip/hip_runtime.h>
#include <hip/hip_bf16.h>
#include <stdint.h>

#define GAS __attribute__((address_space(1)))
#define LAS __attribute__((address_space(3)))
#define DI __device__ __forceinline__
typedef __bf16 bf16x8 __attribute__((ext_vector_type(8)));
typedef __bf16 bf16x2 __attribute__((ext_vector_type(2)));
typedef short s16x4 __attribute__((ext_vector_type(4)));
typedef float f32x16 __attribute__((ext_vector_type(16)));
typedef float f32x4 __attribute__((ext_vector_type(4)));
typedef float f32x2 __attribute__((ext_vector_type(2)));
typedef unsigned u32x4 __attribute__((ext_vector_type(4)));
typedef unsigned u32x2 __attribute__((ext_vector_type(2)));

template <int N> struct IC { static constexpr int value = N; };
template <int B, int E, class F> DI void sfor(F&& f) {
  if constexpr (B < E) { f(IC<B>{}); sfor<B + 1, E>(f); }
}

constexpr unsigned OFF_H = 0;
constexpr unsigned OFF_PQ = 6291456;
constexpr unsigned OFF_WT = 11534336;
constexpr unsigned OFF_KVP = 14319616;
constexpr unsigned OFF_KVT = 16039936;
constexpr unsigned OFF_FLAG = 16105472;
constexpr unsigned WS_BYTES = 16106496;

constexpr int NS = 6;
constexpr int REG = 10368;
constexpr int STG = 2 * REG;
constexpr int L_PT = NS * STG;
constexpr int L_WPHI = L_PT + 12288;
constexpr int L_ZERO = L_WPHI + 7168;
constexpr int L_CSS = L_ZERO + 3328;
constexpr int L_CSF = L_CSS + 5120;
constexpr int K1_LDS = L_CSF + 1280;
constexpr int PK_PITCH = 704, V_PITCH = 336;
constexpr int L_PK = 0, L_V = 128 * PK_PITCH;

DI unsigned pk2(float a, float b) {
  bf16x2 v; v[0] = (__bf16)a; v[1] = (__bf16)b;
  return __builtin_bit_cast(unsigned, v);
}
DI float dot2(unsigned a, unsigned b, float c) {
  return __builtin_amdgcn_fdot2_f32_bf16(__builtin_bit_cast(bf16x2, a), __builtin_bit_cast(bf16x2, b), c, false);
}
DI u32x2 trread(LAS char* p) {
  s16x4 v = __builtin_amdgcn_ds_read_tr16_b64_v4i16((LAS s16x4*)p);
  return __builtin_bit_cast(u32x2, v);
}
DI f32x16 mfma32(u32x4 a, u32x4 b, f32x16 c) {
  return __builtin_amdgcn_mfma_f32_32x32x16_bf16(__builtin_bit_cast(bf16x8, a), __builtin_bit_cast(bf16x8, b), c, 0, 0, 0);
}
DI f32x4 mfma16(u32x4 a, u32x4 b, f32x4 c) {
  return __builtin_amdgcn_mfma_f32_16x16x32_bf16(__builtin_bit_cast(bf16x8, a), __builtin_bit_cast(bf16x8, b), c, 0, 0, 0);
}
DI void dma16(const void* g, int ldsaddr) {
  asm volatile("s_mov_b32 m0, %1\n\ts_nop 0\n\tglobal_load_lds_dwordx4 %0, off" ::"v"(g), "s"(ldsaddr) : "memory");
}
DI void dma16s(unsigned voff, const char* sbase, int ldsaddr) {
  asm volatile("s_mov_b32 m0, %2\n\ts_nop 0\n\tglobal_load_lds_dwordx4 %0, %1" ::"v"(voff), "s"(sbase), "s"(ldsaddr) : "memory");
}
template <int OFF> DI void gload16(u32x4& dst, unsigned voff, const char* sbase) {
  asm volatile("global_load_dwordx4 %0, %1, %2 offset:%3" : "=v"(dst) : "v"(voff), "s"(sbase), "i"(OFF));
}
DI void pinf(float& v) { asm volatile("" : "+v"(v)); }
DI void pin(u32x4& v) { asm volatile("" : "+v"(v)); }
#define WAIT_V(n) asm volatile("s_waitcnt vmcnt(%0)" ::"i"(n) : "memory")
#define BAR() __builtin_amdgcn_s_barrier()
#define SCHED() __builtin_amdgcn_sched_barrier(0)

constexpr int grp_cnt(int j) { return j < 10 ? 9 : (j < 20 ? 5 : 0); }
constexpr int pend_after(int s) {
  int n = 0;
  for (int j = s + 2; j <= s + NS - 2; ++j) n += grp_cnt(j);
  return n;
}

__global__ __launch_bounds__(256, 1) void k1_proj(const __hip_bfloat16* __restrict__ x_, const __hip_bfloat16* __restrict__ wqkv_,
                                                  const __hip_bfloat16* __restrict__ wphi_, const __hip_bfloat16* __restrict__ wo_,
                                                  const __hip_bfloat16* __restrict__ wg_, const __hip_bfloat16* __restrict__ wu_,
                                                  const __hip_bfloat16* __restrict__ wd_, char* __restrict__ ws) {
  extern __shared__ __attribute__((aligned(16))) char smem[];
  LAS char* L = (LAS char*)smem;
  const int lb = (int)(size_t)L;
  const char* x = (const char*)x_;
  const char* wqkv = (const char*)wqkv_;
  const char* wg = (const char*)wg_;
  const char* wu = (const char*)wu_;
  const int tid = threadIdx.x, l = tid & 63;
  const int w = __builtin_amdgcn_readfirstlane(tid >> 6);
  const int r = l & 31, hh = l >> 5;
  const int b = blockIdx.x, xcd = b & 7, idx = b >> 3;
  const int tg = (xcd & 1) * 8 + (idx & 7);
  const int cg = (xcd >> 1) * 4 + (idx >> 3);
  const int ga = cg >> 2, bt = cg & 3;
  const int tok0 = tg * 128 + w * 32;
  const bool aM = bt >= 2, bM = bt >= 1;
  const int mA = min(80 * (4 + 4 * ga + 2 * (bt - 2)), 1456);
  const int mB = (bt == 1) ? 80 * ga : min(80 * (5 + 4 * ga + 2 * (bt - 2)), 1456);
  const int colA = (bt == 0 ? 640 : 0) + 160 * ga;
  const int colB = 1280 + 160 * ga;

  const int rg = w >> 1;
  unsigned voff[5];
  const char* sb;
  int sinc;
  auto make_src = [&](bool isM, int col, int mbase) {
    const int rs = isM ? 3072 : 3840;
#pragma unroll
    for (int i = 0; i < 5; ++i) {
      const int c = 64 * (5 * (w & 1) + i) + l;
      const int row = c / 10, cc = c - row * 10;
      voff[i] = row * rs + 16 * cc;
    }
    sb = isM ? (rg ? wu : wg) + mbase * 2 : wqkv + (col + 80 * rg) * 2;
    sinc = 64 * rs;
  };
  make_src(aM, colA, mA);
  const int ldst = lb + rg * REG + 5 * (w & 1) * 1024;

  unsigned xo[4];
#pragma unroll
  for (int j = 0; j < 4; ++j) xo[j] = (tok0 + r) * 1280 + ((j + w) & 3) * 32 + hh * 16;
  u32x4 xf[40];

  auto issue_op = [&](auto J, auto O) {
    constexpr int j = decltype(J)::value, o = decltype(O)::value;
    constexpr int nx = j < 10 ? 4 : 0;
    if constexpr (j < 20) {
      if constexpr (o < nx) {
        gload16<j * 128>(xf[4 * j + o], xo[o], x);
      } else if constexpr (o < nx + 5) {
        constexpr int i = o - nx;
        if constexpr (j == 10 && i == 0) make_src(bM, colB, mB);
        dma16s(voff[i], sb, ldst + (j % NS) * STG + i * 1024);
        if constexpr (i == 4) sb += sinc;
      }
    }
  };
  sfor<0, NS - 1>([&](auto J) { sfor<0, 9>([&](auto O) { issue_op(J, O); }); });

  const int gw = b * 4 + w;
#pragma unroll
  for (int i = 0; i < 3; ++i) {
    const int fr = gw + 1024 * i;
    if (fr < 2720) {
      const int fg = fr / 340, rem = fr - fg * 340, ks = rem / 5, nt = rem - ks * 5;
      const int slot = l >> 1, g = slot >> 3, j = slot & 7;
      const char* p;
      if (ks < 48) {
        p = (const char*)wd_ + (size_t)(32 * ks + slot) * 1280;
      } else {
        const int G = (ks - 48) / 5, s = (ks - 48) - G * 5;
        const int id = 8 * s + j, e = id / 5, v = id - e * 5;
        const int d = v < 4 ? 4 * g + v : 16 + g;
        p = (const char*)wo_ + (size_t)((8 * G + e) * 20 + d) * 1280;
      }
      dma16(p + (80 * fg + 16 * nt + 8 * (l & 1)) * 2, lb + L_PT + (w * 3 + i) * 1024);
    }
  }
  if (bt <= 1) {
#pragma unroll
    for (int i = 0; i < 2; ++i) {
      const int ii = w * 2 + i;
      if (ii < 7) {
        const int c = min(ii * 64 + l, 399);
        dma16((const char*)wphi_ + (size_t)ga * 6400 + c * 16, lb + L_WPHI + ii * 1024);
      }
    }
    if (tid < 208) *(LAS u32x4*)(L + L_ZERO + tid * 16) = u32x4{0, 0, 0, 0};
  }
  if (b == 0) *(unsigned*)(ws + OFF_FLAG + tid * 4) = 0u;

  const int q = (l & 15) >> 2, p = l & 3, grp = (l >> 4) & 1;
  const int Qi = 4 * grp + p, gq = Qi >> 1, hq = Qi & 1;
  const int colQ = REG * hq + 40 * gq, colM = REG * (gq & 1) + 80 * hq + 40 * (gq >> 1);
  int abase[4];
  auto make_abase = [&](bool isM) {
#pragma unroll
    for (int j = 0; j < 4; ++j) abase[j] = (16 * ((j + w) & 3) + 8 * hh + q) * 160 + (isM ? colM : colQ);
  };
  make_abase(aM);

  f32x16 acc[2][5];
#pragma unroll
  for (int pp = 0; pp < 2; ++pp)
#pragma unroll
    for (int t = 0; t < 5; ++t)
#pragma unroll
      for (int i = 0; i < 16; ++i) acc[pp][t][i] = 0.f;
  float cs[5] = {0.f, 0.f, 0.f, 0.f, 0.f};
  float ssum0 = 0.f, ssum1 = 0.f, ssq0 = 0.f, ssq1 = 0.f;
  const unsigned ONES = 0x3F803F80u;

  u32x4 af[2][5];
  auto readA = [&](auto N, auto T) {
    constexpr int n = decltype(N)::value, t = decltype(T)::value;
    constexpr int s = n / 4, j = n % 4;
    LAS char* bp = L + abase[j] + (s % NS) * STG;
    u32x2 lo = trread(bp + 8 * t), hi = trread(bp + 8 * t + 640);
    af[n & 1][t] = u32x4{lo[0], lo[1], hi[0], hi[1]};
  };

  WAIT_V(36);
  BAR();
  sfor<0, 5>([&](auto T) { readA(IC<0>{}, T); });
  SCHED();

  sfor<0, 80>([&](auto N) {
    constexpr int n = decltype(N)::value;
    constexpr int s = n / 4, j = n % 4, pp = n / 40;
    if constexpr (pp == 0) pin(xf[n]);
    const u32x4 xb = xf[n % 40];
    sfor<0, 5>([&](auto T) {
      constexpr int t = decltype(T)::value;
      acc[pp][t] = mfma32(af[n & 1][t], xb, acc[pp][t]);
      if constexpr (t == 0 && j == 3 && s + 1 < 20) {
        SCHED();
        WAIT_V(pend_after(s));
        BAR();
      }
      if constexpr (t == 0 && n == 39) make_abase(bM);
      if constexpr (n + 1 < 80) readA(IC<n + 1>{}, T);
      if constexpr (j == 3) issue_op(IC<s + NS - 1>{}, T);
      if constexpr (j == 0 && s >= 1 && t < 4) issue_op(IC<s + NS - 2>{}, IC<5 + t>{});
      if constexpr (pp == 0 && t < 4) {
        if constexpr (t & 1) { ssum1 = dot2(xb[t], ONES, ssum1); ssq1 = dot2(xb[t], xb[t], ssq1); pinf(ssum1); pinf(ssq1); }
        else { ssum0 = dot2(xb[t], ONES, ssum0); ssq0 = dot2(xb[t], xb[t], ssq0); pinf(ssum0); pinf(ssq0); }
      }
      if constexpr (j == 0) {
#pragma unroll
        for (int i = 0; i < 4; ++i) cs[t] = dot2(af[n & 1][t][i], ONES, cs[t]);
        pinf(cs[t]);
      }
      SCHED();
    });
    if constexpr (n == 39) {
#pragma unroll
      for (int t = 0; t < 5; ++t) { *(LAS float*)(L + L_CSS + ((2 * w + hh) * 160 + t * 32 + r) * 4) = cs[t]; cs[t] = 0.f; }
    }
    if constexpr (n == 44) {
      if (tid < 160) {
        float a = 0.f;
#pragma unroll
        for (int k = 0; k < 8; ++k) a += *(LAS float*)(L + L_CSS + (k * 160 + tid) * 4);
        *(LAS float*)(L + L_CSF + tid * 4) = a;
      }
    }
  });

#pragma unroll
  for (int t = 0; t < 5; ++t) *(LAS float*)(L + L_CSS + ((2 * w + hh) * 160 + t * 32 + r) * 4) = cs[t];
  WAIT_V(0);
  __syncthreads();
  if (tid < 160) {
    float a = 0.f;
#pragma unroll
    for (int k = 0; k < 8; ++k) a += *(LAS float*)(L + L_CSS + (k * 160 + tid) * 4);
    *(LAS float*)(L + L_CSF + 640 + tid * 4) = a;
  }
#pragma unroll
  for (int i = 0; i < 3; ++i) {
    const int fr = gw + 1024 * i;
    if (fr < 2720) {
      LAS char* bp = L + L_PT + (w * 3 + i) * 1024 + (8 * (l >> 4) + q) * 32 + 8 * p;
      u32x2 lo = trread(bp), hi = trread(bp + 128);
      *(u32x4*)(ws + OFF_WT + (size_t)fr * 1024 + l * 16) = u32x4{lo[0], lo[1], hi[0], hi[1]};
    }
  }
  __syncthreads();

  float ssum = ssum0 + ssum1, ssq = ssq0 + ssq1;
  ssum += __shfl_xor(ssum, 32);
  ssq += __shfl_xor(ssq, 32);
  const float mean = ssum * (1.f / 640.f);
  const float var = fmaxf(ssq * (1.f / 640.f) - mean * mean, 0.f);
  const float rstd = 1.0f / sqrtf(var + 1e-5f);
  const float nmean = -mean;
  const int tg16 = (tok0 + r) >> 4, r16 = r & 15;

  auto csq = [&](int pp, int t, int g) -> f32x4 { return *(LAS f32x4*)(L + L_CSF + (pp * 160 + t * 32 + 8 * g + 4 * hh) * 4); };

  auto epi_M = [&](auto PP, int mbase) {
    constexpr int pp = decltype(PP)::value;
    const float c1 = -2.0f * 0.7978845608028654f * 1.4426950408889634f;
    const float cA = c1 * rstd, cB = c1 * 0.044715f * rstd * rstd * rstd;
    const float irs2 = var + 1e-5f;
    unsigned hp[20];
#pragma unroll
    for (int t = 0; t < 5; ++t) {
#pragma unroll
      for (int sub = 0; sub < 2; ++sub) {
        const f32x4 cg4 = csq(pp, t, 2 * sub), cu4 = csq(pp, t, 2 * sub + 1);
        float hv[4];
#pragma unroll
        for (int c = 0; c < 4; ++c) {
          const float g = fmaf(nmean, cg4[c], acc[pp][t][8 * sub + c]);
          const float u = fmaf(nmean, cu4[c], acc[pp][t][8 * sub + 4 + c]);
          const float arg = fmaf(g * g, cB, cA) * g;
          const float e = __builtin_amdgcn_exp2f(arg);
          const float rr = __builtin_amdgcn_rcpf(fmaf(e, irs2, irs2));
          hv[c] = g * u * rr;
        }
        const int qi = 5 * sub + t;
        hp[2 * qi] = pk2(hv[0], hv[1]);
        hp[2 * qi + 1] = pk2(hv[2], hv[3]);
      }
    }
    char* dst = ws + OFF_H + (size_t)tg16 * 49152 + ((mbase + 40 * hh) >> 3) * 256 + r16 * 16;
#pragma unroll
    for (int m = 0; m < 5; ++m) *(u32x4*)(dst + m * 256) = u32x4{hp[4 * m], hp[4 * m + 1], hp[4 * m + 2], hp[4 * m + 3]};
  };

  auto epi_phi = [&](u32x4 (&out)[4][5]) {
    const bool vA = (hq == hh);
    const int wbA = vA ? (L_WPHI + hh * 3200 + q * 40 + gq * 8) : L_ZERO;
    const int wbR = (vA && gq == 0) ? (L_WPHI + hh * 3200 + q * 40 + 32) : L_ZERO;
    const float c1 = rstd * 1.4426950408889634f;
#pragma unroll
    for (int g = 0; g < 4; ++g) {
      unsigned qp[10];
#pragma unroll
      for (int t = 0; t < 5; ++t) {
        const f32x4 c4 = csq(0, t, g);
        float v[4];
#pragma unroll
        for (int c = 0; c < 4; ++c) v[c] = fmaf(nmean, c4[c], acc[0][t][4 * g + c]);
        qp[2 * t] = pk2(v[0], v[1]);
        qp[2 * t + 1] = pk2(v[2], v[3]);
      }
      f32x16 oa, orr;
#pragma unroll
      for (int i = 0; i < 16; ++i) { oa[i] = 0.f; orr[i] = 0.f; }
#pragma unroll
      for (int s = 0; s < 3; ++s) {
        const u32x4 bq = s < 2 ? u32x4{qp[4 * s], qp[4 * s + 1], qp[4 * s + 2], qp[4 * s + 3]} : u32x4{qp[8], qp[9], 0u, 0u};
        u32x2 a0 = trread(L + wbA + g * 800 + s * 320), r0 = trread(L + wbR + g * 800 + s * 320);
        u32x2 a1 = u32x2{0u, 0u}, r1 = u32x2{0u, 0u};
        if (s < 2) { a1 = trread(L + wbA + g * 800 + s * 320 + 160); r1 = trread(L + wbR + g * 800 + s * 320 + 160); }
        oa = mfma32(u32x4{a0[0], a0[1], a1[0], a1[1]}, bq, oa);
        orr = mfma32(u32x4{r0[0], r0[1], r1[0], r1[1]}, bq, orr);
      }
      float pv[20];
#pragma unroll
      for (int f = 0; f < 16; ++f) pv[f] = oa[f];
#pragma unroll
      for (int f = 0; f < 4; ++f) pv[16 + f] = orr[f];
      float mx = pv[0], mn = pv[0];
#pragma unroll
      for (int f = 1; f < 20; ++f) { mx = fmaxf(mx, pv[f]); mn = fminf(mn, pv[f]); }
      const float bx = -mx * c1, bn = mn * c1;
      float ep[20], en[20], sp = 0.f, sn = 0.f;
#pragma unroll
      for (int f = 0; f < 20; ++f) {
        ep[f] = __builtin_amdgcn_exp2f(fmaf(pv[f], c1, bx));
        en[f] = __builtin_amdgcn_exp2f(fmaf(pv[f], -c1, bn));
        sp += ep[f]; sn += en[f];
      }
      const float rp = __builtin_amdgcn_rcpf(sp), rn = __builtin_amdgcn_rcpf(sn);
      unsigned o[20];
#pragma unroll
      for (int f = 0; f < 10; ++f) {
        o[f] = pk2(ep[2 * f] * rp, ep[2 * f + 1] * rp);
        o[10 + f] = pk2(en[2 * f] * rn, en[2 * f + 1] * rn);
      }
#pragma unroll
      for (int ci = 0; ci < 5; ++ci) out[g][ci] = u32x4{o[4 * ci], o[4 * ci + 1], o[4 * ci + 2], o[4 * ci + 3]};
    }
  };

  if (bt >= 2) {
    epi_M(IC<0>{}, mA);
    epi_M(IC<1>{}, mB);
  } else if (bt == 1) {
    u32x4 ph[4][5];
    epi_phi(ph);
    char* dst = ws + OFF_PQ + (size_t)(tg16 * 8 + 2 * ga + hh) * 5120 + r16 * 16;
#pragma unroll
    for (int g = 0; g < 4; ++g) {
#pragma unroll
      for (int ci = 0; ci < 4; ++ci) *(u32x4*)(dst + g * 1024 + ci * 256) = ph[g][ci];
      *(u32x4*)(dst + 4096 + g * 256) = ph[g][4];
    }
    epi_M(IC<1>{}, mB);
  } else {
    u32x4 ph[4][5];
    epi_phi(ph);
    const int trow = 32 * w + r;
#pragma unroll
    for (int g = 0; g < 4; ++g)
#pragma unroll
      for (int ci = 0; ci < 5; ++ci) *(LAS u32x4*)(L + L_PK + trow * PK_PITCH + (4 * hh + g) * 80 + ci * 16) = ph[g][ci];
#pragma unroll
    for (int g = 0; g < 4; ++g)
#pragma unroll
      for (int t = 0; t < 5; ++t) {
        const f32x4 c4 = csq(1, t, g);
        float v[4];
#pragma unroll
        for (int c = 0; c < 4; ++c) v[c] = fmaf(nmean, c4[c], acc[1][t][4 * g + c]) * rstd;
        *(LAS u32x2*)(L + L_V + trow * V_PITCH + (4 * hh + g) * 40 + 8 * t) = u32x2{pk2(v[0], v[1]), pk2(v[2], v[3])};
      }
    if (hh == 0) *(LAS u32x2*)(L + L_V + trow * V_PITCH + 320) = u32x2{0x00003F80u, 0u};
    __syncthreads();
#pragma unroll
    for (int ee = 0; ee < 2; ++ee) {
      const int e = 2 * w + ee;
      const int rowoff = 8 * hh + q;
      LAS char* pa0 = L + L_PK + rowoff * PK_PITCH + e * 80 + 32 * grp + 8 * p;
      LAS char* pa1 = L + L_PK + rowoff * PK_PITCH + e * 80 + 64 + 8 * (p & 1);
      LAS char* pb = L + L_V + rowoff * V_PITCH + (grp == 0 ? e * 40 + 8 * p : (p == 0 ? e * 40 + 32 : 320));
      f32x16 d0, d1;
#pragma unroll
      for (int i = 0; i < 16; ++i) { d0[i] = 0.f; d1[i] = 0.f; }
#pragma unroll
      for (int ks = 0; ks < 8; ++ks) {
        u32x2 a0 = trread(pa0 + ks * 16 * PK_PITCH), a1 = trread(pa0 + (ks * 16 + 4) * PK_PITCH);
        u32x2 c0 = trread(pa1 + ks * 16 * PK_PITCH), c1 = trread(pa1 + (ks * 16 + 4) * PK_PITCH);
        u32x2 b0 = trread(pb + ks * 16 * V_PITCH), b1 = trread(pb + (ks * 16 + 4) * V_PITCH);
        const u32x4 bb = u32x4{b0[0], b0[1], b1[0], b1[1]};
        d0 = mfma32(u32x4{a0[0], a0[1], a1[0], a1[1]}, bb, d0);
        d1 = mfma32(u32x4{c0[0], c0[1], c1[0], c1[1]}, bb, d1);
      }
      if (r <= 20) {
        float* dst = (float*)(ws + OFF_KVP) + ((size_t)(tg * 32 + 8 * ga + e) * 21 + r) * 40 + 4 * hh;
#pragma unroll
        for (int g = 0; g < 4; ++g) *(f32x4*)(dst + 8 * g) = f32x4{d0[4 * g], d0[4 * g + 1], d0[4 * g + 2], d0[4 * g + 3]};
        *(f32x4*)(dst + 32) = f32x4{d1[0], d1[1], d1[2], d1[3]};
      }
    }
  }
}

constexpr int K2_LDS = 163840;
__global__ __launch_bounds__(512, 2) void k2_out(const __hip_bfloat16* __restrict__ x_, char* __restrict__ ws,
                                                 __hip_bfloat16* __restrict__ out_) {
  extern __shared__ __attribute__((aligned(16))) char smem[];
  LAS char* L = (LAS char*)smem;
  const int tid = threadIdx.x, l = tid & 63;
  const int w = __builtin_amdgcn_readfirstlane(tid >> 6);
  const int b = blockIdx.x, xcd = b & 7, idx = b >> 3;
  const int tg = xcd * 4 + (idx & 3);
  const int fg = idx >> 2;
  const __amdgpu_buffer_rsrc_t rs = __builtin_amdgcn_make_buffer_rsrc((void*)ws, 0, (int)WS_BYTES, 0x00020000);
  const GAS u32x4* wt = (const GAS u32x4*)(ws + OFF_WT + (size_t)fg * 68 * 5120) + l;
  f32x4 acc[5][4];
#pragma unroll
  for (int nt = 0; nt < 5; ++nt)
#pragma unroll
    for (int i = 0; i < 4; ++i) acc[nt][i] = f32x4{0.f, 0.f, 0.f, 0.f};

  if (w < 4) {
    const GAS u32x4* hb = (const GAS u32x4*)(ws + OFF_H + (size_t)(tg * 4) * 49152 + (size_t)(12 * w) * 1024) + l;
    const GAS u32x4* wa = wt + (size_t)(12 * w) * 320;
    constexpr int WIN = 4;
    u32x4 fa[WIN][5], fb[WIN][4];
    auto ld = [&](auto K) {
      constexpr int k = decltype(K)::value;
#pragma unroll
      for (int i = 0; i < 4; ++i) fb[k % WIN][i] = hb[i * 3072 + k * 64];
#pragma unroll
      for (int nt = 0; nt < 5; ++nt) fa[k % WIN][nt] = wa[k * 320 + nt * 64];
    };
    sfor<0, WIN>(ld);
    __builtin_amdgcn_sched_barrier(0);
    sfor<0, 12>([&](auto K) {
      constexpr int k = decltype(K)::value;
      u32x4 ca[5], cb[4];
#pragma unroll
      for (int nt = 0; nt < 5; ++nt) ca[nt] = fa[k % WIN][nt];
#pragma unroll
      for (int i = 0; i < 4; ++i) cb[i] = fb[k % WIN][i];
#pragma unroll
      for (int nt = 0; nt < 5; ++nt)
#pragma unroll
        for (int i = 0; i < 4; ++i) acc[nt][i] = mfma16(ca[nt], cb[i], acc[nt][i]);
      __builtin_amdgcn_sched_barrier(0);
      if constexpr (k + WIN < 12) ld(IC<k + WIN>{});
      __builtin_amdgcn_sched_barrier(0);
    });
  } else {
    const int G = w - 4;
    if (w == 4 && b < 210) {
      const GAS f32x2* pp = (const GAS f32x2*)(ws + OFF_KVP) + b * 64 + l;
      f32x2 v[16];
#pragma unroll
      for (int k = 0; k < 16; ++k) v[k] = pp[k * 13440];
      f32x2 s = v[0];
#pragma unroll
      for (int k = 1; k < 16; ++k) s += v[k];
      __builtin_amdgcn_raw_buffer_store_b32(pk2(s[0], s[1]), rs, (int)OFF_KVT + b * 256 + l * 4, 0, 16);
      asm volatile("s_waitcnt vmcnt(0)" ::: "memory");
      if (l == 0) __hip_atomic_store((GAS unsigned*)(ws + OFF_FLAG) + b, 1u, __ATOMIC_RELAXED, __HIP_MEMORY_SCOPE_AGENT);
    }
    const GAS u32x4* pq = (const GAS u32x4*)(ws + OFF_PQ + (size_t)(tg * 4 * 8 + 2 * G) * 5120) + l;
    u32x4 pf[2][4][5];
#pragma unroll
    for (int u = 0; u < 2; ++u)
#pragma unroll
      for (int i = 0; i < 4; ++i)
#pragma unroll
        for (int f = 0; f < 5; ++f)
          if (!(u == 1 && (f == 2 || f == 3))) pf[u][i][f] = pq[i * 2560 + u * 320 + f * 64];
    __builtin_amdgcn_sched_barrier(0);
    {
      GAS unsigned* fl = (GAS unsigned*)(ws + OFF_FLAG);
      for (unsigned spin = 0; spin < (1u << 20); ++spin) {
        bool ok = true;
#pragma unroll
        for (int k = 0; k < 4; ++k) {
          const int id = k * 64 + l;
          const unsigned v = __hip_atomic_load(fl + (id < 210 ? id : 0), __ATOMIC_RELAXED, __HIP_MEMORY_SCOPE_AGENT);
          ok = ok && (v == 1u);
        }
        if (__all(ok)) break;
        __builtin_amdgcn_s_sleep(2);
      }
      __builtin_amdgcn_fence(__ATOMIC_ACQUIRE, "wavefront");
    }
    const int r = l & 15, g = l >> 4;
    const int row1 = (r & 3) == 0 ? 16 + (r >> 2) : 20;
    const GAS u32x4* wa = wt + (size_t)(48 + 5 * G) * 320;
    u32x4 ka[2][2][4];
    unsigned op[4][20];
    u32x4 ca[5][5];
    auto ldk = [&](auto HP) {
      constexpr int hp = decltype(HP)::value;
#pragma unroll
      for (int z = 0; z < 2; ++z) {
        const int hd = (2 * hp + z) & 3;
        const int hb = (int)OFF_KVT + (8 * G + 2 * hp + z) * 1680;
        ka[hp & 1][z][0] = __builtin_amdgcn_raw_buffer_load_b128(rs, hb + r * 80 + g * 16, 0, 16);
        ka[hp & 1][z][1] = __builtin_amdgcn_raw_buffer_load_b128(rs, hb + row1 * 80 + g * 16, 0, 16);
        ka[hp & 1][z][2] = __builtin_amdgcn_raw_buffer_load_b128(rs, g == hd ? hb + r * 80 + 64 : 0x7ffffff0, 0, 16);
        ka[hp & 1][z][3] = __builtin_amdgcn_raw_buffer_load_b128(rs, g == hd ? hb + row1 * 80 + 64 : 0x7ffffff0, 0, 16);
      }
    };
    auto ldw = [&](auto S) {
      constexpr int s = decltype(S)::value;
#pragma unroll
      for (int nt = 0; nt < 5; ++nt) ca[s][nt] = wa[s * 320 + nt * 64];
    };
    ldk(IC<0>{});
    ldk(IC<1>{});
    __builtin_amdgcn_sched_barrier(0);
    sfor<0, 4>([&](auto HP) {
      constexpr int hp = decltype(HP)::value;
      constexpr int u = hp >> 1;
#pragma unroll
      for (int i = 0; i < 4; ++i) {
        float ov[10];
#pragma unroll
        for (int z = 0; z < 2; ++z) {
          const int hd = (2 * hp + z) & 3;
          f32x4 o0 = f32x4{0.f, 0.f, 0.f, 0.f}, o1 = f32x4{0.f, 0.f, 0.f, 0.f};
          o0 = mfma16(ka[hp & 1][z][0], pf[u][i][hd], o0);
          o1 = mfma16(ka[hp & 1][z][1], pf[u][i][hd], o1);
          o0 = mfma16(ka[hp & 1][z][2], pf[u][i][4], o0);
          o1 = mfma16(ka[hp & 1][z][3], pf[u][i][4], o1);
          const float inv = __builtin_amdgcn_rcpf(o1[1] + 1e-6f);
#pragma unroll
          for (int v = 0; v < 4; ++v) ov[5 * z + v] = o0[v] * inv;
          ov[5 * z + 4] = o1[0] * inv;
        }
#pragma unroll
        for (int j = 0; j < 5; ++j) op[i][5 * hp + j] = pk2(ov[2 * j], ov[2 * j + 1]);
      }
      __builtin_amdgcn_sched_barrier(0);
      if constexpr (hp + 2 < 4) ldk(IC<hp + 2>{});
      if constexpr (hp == 0) {
#pragma unroll
        for (int i = 0; i < 4; ++i) { pf[1][i][2] = pq[i * 2560 + 320 + 128]; pf[1][i][3] = pq[i * 2560 + 320 + 192]; }
      }
      if constexpr (hp == 1) { ldw(IC<0>{}); ldw(IC<1>{}); }
      if constexpr (hp == 2) { ldw(IC<2>{}); ldw(IC<3>{}); }
      if constexpr (hp == 3) { ldw(IC<4>{}); }
      __builtin_amdgcn_sched_barrier(0);
    });
#pragma unroll
    for (int s = 0; s < 5; ++s) {
#pragma unroll
      for (int i = 0; i < 4; ++i) {
        const u32x4 ob = u32x4{op[i][4 * s], op[i][4 * s + 1], op[i][4 * s + 2], op[i][4 * s + 3]};
#pragma unroll
        for (int nt = 0; nt < 5; ++nt) acc[nt][i] = mfma16(ca[s][nt], ob, acc[nt][i]);
      }
    }
  }
#pragma unroll
  for (int nt = 0; nt < 5; ++nt)
#pragma unroll
    for (int i = 0; i < 4; ++i) *(LAS f32x4*)(L + ((w * 20 + nt * 4 + i) * 64 + l) * 16) = acc[nt][i];
  __syncthreads();
#pragma unroll
  for (int k = 0; k < 3; ++k) {
    const int t = w + 8 * k;
    if (t < 20) {
      f32x4 s = *(LAS f32x4*)(L + (t * 64 + l) * 16);
#pragma unroll
      for (int ww = 1; ww < 8; ++ww) s += *(LAS f32x4*)(L + ((ww * 20 + t) * 64 + l) * 16);
      const int nt = t >> 2, i = t & 3;
      const size_t off = ((size_t)(tg * 64 + i * 16 + (l & 15)) * 640 + fg * 80 + nt * 16 + 4 * (l >> 4)) * 2;
      const u32x2 xv = *(const u32x2*)((const char*)x_ + off);
      const float x0 = __builtin_bit_cast(float, xv[0] << 16), x1 = __builtin_bit_cast(float, xv[0] & 0xffff0000u);
      const float x2 = __builtin_bit_cast(float, xv[1] << 16), x3 = __builtin_bit_cast(float, xv[1] & 0xffff0000u);
      *(u32x2*)((char*)out_ + off) = u32x2{pk2(s[0] + x0, s[1] + x1), pk2(s[2] + x2, s[3] + x3)};
    }
  }
}

extern "C" void kernel_launch(void* const* d_in, const int* in_sizes, int n_in, void* d_out, int out_size, void* d_ws,
                              size_t ws_size, hipStream_t stream) {
  const __hip_bfloat16* x = (const __hip_bfloat16*)d_in[0];
  const __hip_bfloat16* wqkv = (const __hip_bfloat16*)d_in[1];
  const __hip_bfloat16* wphi = (const __hip_bfloat16*)d_in[2];
  const __hip_bfloat16* wo = (const __hip_bfloat16*)d_in[3];
  const __hip_bfloat16* wg = (const __hip_bfloat16*)d_in[5];
  const __hip_bfloat16* wu = (const __hip_bfloat16*)d_in[6];
  const __hip_bfloat16* wd = (const __hip_bfloat16*)d_in[7];
  (void)hipFuncSetAttribute((const void*)k1_proj, hipFuncAttributeMaxDynamicSharedMemorySize, K1_LDS);
  (void)hipFuncSetAttribute((const void*)k2_out, hipFuncAttributeMaxDynamicSharedMemorySize, K2_LDS);
  k1_proj<<<256, 256, K1_LDS, stream>>>(x, wqkv, wphi, wo, wg, wu, wd, (char*)d_ws);
  k2_out<<<256, 512, K2_LDS, stream>>>(x, (char*)d_ws, (__hip_bfloat16*)d_out);
}
```

```cpp
#include <hip/hip_runtime.h>
#include <hip/hip_bf16.h>
#include <stdint.h>

#define GAS __attribute__((address_space(1)))
#define LAS __attribute__((address_space(3)))
#define DI __device__ __forceinline__
typedef __bf16 bf16x8 __attribute__((ext_vector_type(8)));
typedef __bf16 bf16x2 __attribute__((ext_vector_type(2)));
typedef short s16x4 __attribute__((ext_vector_type(4)));
typedef float f32x16 __attribute__((ext_vector_type(16)));
typedef float f32x4 __attribute__((ext_vector_type(4)));
typedef float f32x2 __attribute__((ext_vector_type(2)));
typedef unsigned u32x4 __attribute__((ext_vector_type(4)));
typedef unsigned u32x2 __attribute__((ext_vector_type(2)));

template <int N> struct IC { static constexpr int value = N; };
template <int B, int E, class F> DI void sfor(F&& f) {
  if constexpr (B < E) { f(IC<B>{}); sfor<B + 1, E>(f); }
}

constexpr unsigned OFF_H = 0;
constexpr unsigned OFF_PQ = 6291456;
constexpr unsigned OFF_WT = 11534336;
constexpr unsigned OFF_KVP = 14319616;
constexpr unsigned OFF_KVT = 16039936;
constexpr unsigned OFF_FLAG = 16105472;
constexpr unsigned WS_BYTES = 16106496;

constexpr int NS = 6;
constexpr int REG = 10368;
constexpr int STG = 2 * REG;
constexpr int L_PT = NS * STG;
constexpr int L_WPHI = L_PT + 12288;
constexpr int L_ZERO = L_WPHI + 7168;
constexpr int L_CSS = L_ZERO + 3328;
constexpr int L_CSF = L_CSS + 5120;
constexpr int K1_LDS = L_CSF + 1280;
constexpr int PK_PITCH = 704, V_PITCH = 336;
constexpr int L_PK = 0, L_V = 128 * PK_PITCH;

DI unsigned pk2(float a, float b) {
  bf16x2 v; v[0] = (__bf16)a; v[1] = (__bf16)b;
  return __builtin_bit_cast(unsigned, v);
}
DI float dot2(unsigned a, unsigned b, float c) {
  return __builtin_amdgcn_fdot2_f32_bf16(__builtin_bit_cast(bf16x2, a), __builtin_bit_cast(bf16x2, b), c, false);
}
DI u32x2 trread(LAS char* p) {
  s16x4 v = __builtin_amdgcn_ds_read_tr16_b64_v4i16((LAS s16x4*)p);
  return __builtin_bit_cast(u32x2, v);
}
DI f32x16 mfma32(u32x4 a, u32x4 b, f32x16 c) {
  return __builtin_amdgcn_mfma_f32_32x32x16_bf16(__builtin_bit_cast(bf16x8, a), __builtin_bit_cast(bf16x8, b), c, 0, 0, 0);
}
DI f32x4 mfma16(u32x4 a, u32x4 b, f32x4 c) {
  return __builtin_amdgcn_mfma_f32_16x16x32_bf16(__builtin_bit_cast(bf16x8, a), __builtin_bit_cast(bf16x8, b), c, 0, 0, 0);
}
DI void dma16(const void* g, int ldsaddr) {
  asm volatile("s_mov_b32 m0, %1\n\ts_nop 0\n\tglobal_load_lds_dwordx4 %0, off" ::"v"(g), "s"(ldsaddr) : "memory");
}
DI void dma16s(unsigned voff, const char* sbase, int ldsaddr) {
  asm volatile("s_mov_b32 m0, %2\n\ts_nop 0\n\tglobal_load_lds_dwordx4 %0, %1" ::"v"(voff), "s"(sbase), "s"(ldsaddr) : "memory");
}
template <int OFF> DI void gload16(u32x4& dst, unsigned voff, const char* sbase) {
  asm volatile("global_load_dwordx4 %0, %1, %2 offset:%3" : "=v"(dst) : "v"(voff), "s"(sbase), "i"(OFF));
}
DI void pinf(float& v) { asm volatile("" : "+v"(v)); }
DI void pin(u32x4& v) { asm volatile("" : "+v"(v)); }
#define WAIT_V(n) asm volatile("s_waitcnt vmcnt(%0)" ::"i"(n) : "memory")
#define STAMP_DECL
#define STAMP(k, i)
#define STAMP_FLUSH(k)
template <int NL> DI unsigned code_prefetch(int idx, int tid) {
  unsigned long long pc;
  asm volatile("s_getpc_b64 %0" : "=s"(pc));
  unsigned v = 0;
  if (tid < NL) v = *(const GAS unsigned*)(pc + (unsigned long long)(idx * NL + tid) * 64);
  return v;
}
DI const char* uptr(const char* p) {
  const unsigned long long v = (unsigned long long)p;
  const unsigned lo = __builtin_amdgcn_readfirstlane((unsigned)v), hi = __builtin_amdgcn_readfirstlane((unsigned)(v >> 32));
  return (const char*)(((unsigned long long)hi << 32) | lo);
}
#define BAR() __builtin_amdgcn_s_barrier()
#define SCHED() __builtin_amdgcn_sched_barrier(0)

constexpr int grp_cnt(int j) { return j < 10 ? 9 : (j < 20 ? 5 : 0); }
constexpr int pend_after(int s) {
  int n = 0;
  for (int j = s + 2; j <= s + NS - 2; ++j) n += grp_cnt(j);
  return n;
}

__global__ __launch_bounds__(256, 1) void k1_proj(const __hip_bfloat16* __restrict__ x_, const __hip_bfloat16* __restrict__ wqkv_,
                                                  const __hip_bfloat16* __restrict__ wphi_, const __hip_bfloat16* __restrict__ wo_,
                                                  const __hip_bfloat16* __restrict__ wg_, const __hip_bfloat16* __restrict__ wu_,
                                                  const __hip_bfloat16* __restrict__ wd_, char* __restrict__ ws) {
  extern __shared__ __attribute__((aligned(16))) char smem[];
  LAS char* L = (LAS char*)smem;
  const int lb = (int)(size_t)L;
  STAMP_DECL;
  STAMP(0, 0);
  const char* x = (const char*)x_;
  const char* wqkv = (const char*)wqkv_;
  const char* wg = (const char*)wg_;
  const char* wu = (const char*)wu_;
  const int tid = threadIdx.x, l = tid & 63;
  const int w = __builtin_amdgcn_readfirstlane(tid >> 6);
  const int r = l & 31, hh = l >> 5;
  const int b = blockIdx.x, xcd = b & 7, idx = b >> 3;
  const unsigned cpf = code_prefetch<31>(idx, tid);
  const int tg = (xcd & 1) * 8 + (idx & 7);
  const int cg = (xcd >> 1) * 4 + (idx >> 3);
  const int ga = cg >> 2, bt = cg & 3;
  const int tok0 = tg * 128 + w * 32;
  const bool aM = bt >= 2, bM = bt >= 1;
  const int mA = min(80 * (4 + 4 * ga + 2 * (bt - 2)), 1456);
  const int mB = (bt == 1) ? 80 * ga : min(80 * (5 + 4 * ga + 2 * (bt - 2)), 1456);
  const int colA = (bt == 0 ? 640 : 0) + 160 * ga;
  const int colB = 1280 + 160 * ga;

  const int rg = w >> 1;
  unsigned voff[5];
  const char* sb;
  int sinc;
  auto make_src = [&](bool isM, int col, int mbase) {
    const int rs = isM ? 3072 : 3840;
#pragma unroll
    for (int i = 0; i < 5; ++i) {
      const int c = 64 * (5 * (w & 1) + i) + l;
      const int row = c / 10, cc = c - row * 10;
      voff[i] = row * rs + 16 * cc;
    }
    sb = uptr(isM ? (rg ? wu : wg) + mbase * 2 : wqkv + (col + 80 * rg) * 2);
    sinc = 64 * rs;
  };
  make_src(aM, colA, mA);
  const int ldst = lb + rg * REG + 5 * (w & 1) * 1024;

  unsigned xo[4];
#pragma unroll
  for (int j = 0; j < 4; ++j) xo[j] = (tok0 + r) * 1280 + ((j + w) & 3) * 32 + hh * 16;
  u32x4 xf[40];

  auto issue_op = [&](auto J, auto O) {
    constexpr int j = decltype(J)::value, o = decltype(O)::value;
    constexpr int nx = j < 10 ? 4 : 0;
    if constexpr (j < 20) {
      if constexpr (o < nx) {
        gload16<j * 128>(xf[4 * j + o], xo[o], x);
      } else if constexpr (o < nx + 5) {
        constexpr int i = o - nx;
        if constexpr (j == 10 && i == 0) make_src(bM, colB, mB);
        dma16s(voff[i], sb, ldst + (j % NS) * STG + i * 1024);
        if constexpr (i == 4) sb += sinc;
      }
    }
  };
  sfor<0, NS - 1>([&](auto J) { sfor<0, 9>([&](auto O) { issue_op(J, O); }); });

  const int gw = b * 4 + w;
#pragma unroll
  for (int i = 0; i < 3; ++i) {
    const int fr = gw + 1024 * i;
    if (fr < 2720) {
      const int fg = fr / 340, rem = fr - fg * 340, ks = rem / 5, nt = rem - ks * 5;
      const int slot = l >> 1, g = slot >> 3, j = slot & 7;
      const char* p;
      if (ks < 48) {
        p = (const char*)wd_ + (size_t)(32 * ks + slot) * 1280;
      } else {
        const int G = (ks - 48) / 5, s = (ks - 48) - G * 5;
        const int id = 8 * s + j, e = id / 5, v = id - e * 5;
        const int d = v < 4 ? 4 * g + v : 16 + g;
        p = (const char*)wo_ + (size_t)((8 * G + e) * 20 + d) * 1280;
      }
      dma16(p + (80 * fg + 16 * nt + 8 * (l & 1)) * 2, lb + L_PT + (w * 3 + i) * 1024);
    }
  }
  if (bt <= 1) {
#pragma unroll
    for (int i = 0; i < 2; ++i) {
      const int ii = w * 2 + i;
      if (ii < 7) {
        const int c = min(ii * 64 + l, 399);
        dma16((const char*)wphi_ + (size_t)ga * 6400 + c * 16, lb + L_WPHI + ii * 1024);
      }
    }
    if (tid < 208) *(LAS u32x4*)(L + L_ZERO + tid * 16) = u32x4{0, 0, 0, 0};
  }
  if (b == 0) *(unsigned*)(ws + OFF_FLAG + tid * 4) = 0u;

  const int q = (l & 15) >> 2, p = l & 3, grp = (l >> 4) & 1;
  const int Qi = 4 * grp + p, gq = Qi >> 1, hq = Qi & 1;
  const int colQ = REG * hq + 40 * gq, colM = REG * (gq & 1) + 80 * hq + 40 * (gq >> 1);
  int abase[4];
  auto make_abase = [&](bool isM) {
#pragma unroll
    for (int j = 0; j < 4; ++j) abase[j] = (16 * ((j + w) & 3) + 8 * hh + q) * 160 + (isM ? colM : colQ);
  };
  make_abase(aM);

  f32x16 acc[2][5];
#pragma unroll
  for (int pp = 0; pp < 2; ++pp)
#pragma unroll
    for (int t = 0; t < 5; ++t)
#pragma unroll
      for (int i = 0; i < 16; ++i) acc[pp][t][i] = 0.f;
  float cs[5] = {0.f, 0.f, 0.f, 0.f, 0.f};
  float ssum0 = 0.f, ssum1 = 0.f, ssq0 = 0.f, ssq1 = 0.f;
  const unsigned ONES = 0x3F803F80u;

  u32x4 af[2][5];
  auto readA = [&](auto N, auto T) {
    constexpr int n = decltype(N)::value, t = decltype(T)::value;
    constexpr int s = n / 4, j = n % 4;
    LAS char* bp = L + abase[j] + (s % NS) * STG;
    u32x2 lo = trread(bp + 8 * t), hi = trread(bp + 8 * t + 640);
    af[n & 1][t] = u32x4{lo[0], lo[1], hi[0], hi[1]};
  };

  STAMP(0, 1);
  WAIT_V(36);
  BAR();
  STAMP(0, 2);
  sfor<0, 5>([&](auto T) { readA(IC<0>{}, T); });
  SCHED();

  sfor<0, 80>([&](auto N) {
    constexpr int n = decltype(N)::value;
    constexpr int s = n / 4, j = n % 4, pp = n / 40;
    if constexpr (pp == 0) pin(xf[n]);
    const u32x4 xb = xf[n % 40];
    sfor<0, 5>([&](auto T) {
      constexpr int t = decltype(T)::value;
      acc[pp][t] = mfma32(af[n & 1][t], xb, acc[pp][t]);
      if constexpr (t == 0 && j == 3 && s + 1 < 20) {
        SCHED();
        WAIT_V(pend_after(s));
        BAR();
      }
      if constexpr (t == 0 && n == 39) make_abase(bM);
      if constexpr (n + 1 < 80) readA(IC<n + 1>{}, T);
      if constexpr (j == 3) issue_op(IC<s + NS - 1>{}, T);
      if constexpr (j == 0 && s >= 1 && t < 4) issue_op(IC<s + NS - 2>{}, IC<5 + t>{});
      if constexpr (pp == 0 && t < 4) {
        if constexpr (t & 1) { ssum1 = dot2(xb[t], ONES, ssum1); ssq1 = dot2(xb[t], xb[t], ssq1); pinf(ssum1); pinf(ssq1); }
        else { ssum0 = dot2(xb[t], ONES, ssum0); ssq0 = dot2(xb[t], xb[t], ssq0); pinf(ssum0); pinf(ssq0); }
      }
      if constexpr (j == 0) {
#pragma unroll
        for (int i = 0; i < 4; ++i) cs[t] = dot2(af[n & 1][t][i], ONES, cs[t]);
        pinf(cs[t]);
      }
      SCHED();
    });
    if constexpr (n == 19) STAMP(0, 3);
    if constexpr (n == 39) STAMP(0, 4);
    if constexpr (n == 59) STAMP(0, 5);
    if constexpr (n == 39) {
#pragma unroll
      for (int t = 0; t < 5; ++t) { *(LAS float*)(L + L_CSS + ((2 * w + hh) * 160 + t * 32 + r) * 4) = cs[t]; cs[t] = 0.f; }
    }
    if constexpr (n == 44) {
      if (tid < 160) {
        float a = 0.f;
#pragma unroll
        for (int k = 0; k < 8; ++k) a += *(LAS float*)(L + L_CSS + (k * 160 + tid) * 4);
        *(LAS float*)(L + L_CSF + tid * 4) = a;
      }
    }
  });

  STAMP(0, 6);
#pragma unroll
  for (int t = 0; t < 5; ++t) *(LAS float*)(L + L_CSS + ((2 * w + hh) * 160 + t * 32 + r) * 4) = cs[t];
  WAIT_V(0);
  __syncthreads();
  if (tid < 160) {
    float a = 0.f;
#pragma unroll
    for (int k = 0; k < 8; ++k) a += *(LAS float*)(L + L_CSS + (k * 160 + tid) * 4);
    *(LAS float*)(L + L_CSF + 640 + tid * 4) = a;
  }
#pragma unroll
  for (int i = 0; i < 3; ++i) {
    const int fr = gw + 1024 * i;
    if (fr < 2720) {
      LAS char* bp = L + L_PT + (w * 3 + i) * 1024 + (8 * (l >> 4) + q) * 32 + 8 * p;
      u32x2 lo = trread(bp), hi = trread(bp + 128);
      *(u32x4*)(ws + OFF_WT + (size_t)fr * 1024 + l * 16) = u32x4{lo[0], lo[1], hi[0], hi[1]};
    }
  }
  __syncthreads();

  STAMP(0, 7);
  float ssum = ssum0 + ssum1, ssq = ssq0 + ssq1;
  ssum += __shfl_xor(ssum, 32);
  ssq += __shfl_xor(ssq, 32);
  const float mean = ssum * (1.f / 640.f);
  const float var = fmaxf(ssq * (1.f / 640.f) - mean * mean, 0.f);
  const float rstd = 1.0f / sqrtf(var + 1e-5f);
  const float nmean = -mean;
  const int tg16 = (tok0 + r) >> 4, r16 = r & 15;

  auto csq = [&](int pp, int t, int g) -> f32x4 { return *(LAS f32x4*)(L + L_CSF + (pp * 160 + t * 32 + 8 * g + 4 * hh) * 4); };

  auto epi_M = [&](auto PP, int mbase) {
    constexpr int pp = decltype(PP)::value;
    const float c1 = -2.0f * 0.7978845608028654f * 1.4426950408889634f;
    const float cA = c1 * rstd, cB = c1 * 0.044715f * rstd * rstd * rstd;
    const float irs2 = var + 1e-5f;
    unsigned hp[20];
#pragma unroll
    for (int t = 0; t < 5; ++t) {
#pragma unroll
      for (int sub = 0; sub < 2; ++sub) {
        const f32x4 cg4 = csq(pp, t, 2 * sub), cu4 = csq(pp, t, 2 * sub + 1);
        float hv[4];
#pragma unroll
        for (int c = 0; c < 4; ++c) {
          const float g = fmaf(nmean, cg4[c], acc[pp][t][8 * sub + c]);
          const float u = fmaf(nmean, cu4[c], acc[pp][t][8 * sub + 4 + c]);
          const float arg = fmaf(g * g, cB, cA) * g;
          const float e = __builtin_amdgcn_exp2f(arg);
          const float rr = __builtin_amdgcn_rcpf(fmaf(e, irs2, irs2));
          hv[c] = g * u * rr;
        }
        const int qi = 5 * sub + t;
        hp[2 * qi] = pk2(hv[0], hv[1]);
        hp[2 * qi + 1] = pk2(hv[2], hv[3]);
      }
    }
    char* dst = ws + OFF_H + (size_t)tg16 * 49152 + ((mbase + 40 * hh) >> 3) * 256 + r16 * 16;
#pragma unroll
    for (int m = 0; m < 5; ++m) *(u32x4*)(dst + m * 256) = u32x4{hp[4 * m], hp[4 * m + 1], hp[4 * m + 2], hp[4 * m + 3]};
  };

  auto epi_phi = [&](u32x4 (&out)[4][5]) {
    const bool vA = (hq == hh);
    const int wbA = vA ? (L_WPHI + hh * 3200 + q * 40 + gq * 8) : L_ZERO;
    const int wbR = (vA && gq == 0) ? (L_WPHI + hh * 3200 + q * 40 + 32) : L_ZERO;
    const float c1 = rstd * 1.4426950408889634f;
#pragma unroll
    for (int g = 0; g < 4; ++g) {
      unsigned qp[10];
#pragma unroll
      for (int t = 0; t < 5; ++t) {
        const f32x4 c4 = csq(0, t, g);
        float v[4];
#pragma unroll
        for (int c = 0; c < 4; ++c) v[c] = fmaf(nmean, c4[c], acc[0][t][4 * g + c]);
        qp[2 * t] = pk2(v[0], v[1]);
        qp[2 * t + 1] = pk2(v[2], v[3]);
      }
      f32x16 oa, orr;
#pragma unroll
      for (int i = 0; i < 16; ++i) { oa[i] = 0.f; orr[i] = 0.f; }
#pragma unroll
      for (int s = 0; s < 3; ++s) {
        const u32x4 bq = s < 2 ? u32x4{qp[4 * s], qp[4 * s + 1], qp[4 * s + 2], qp[4 * s + 3]} : u32x4{qp[8], qp[9], 0u, 0u};
        u32x2 a0 = trread(L + wbA + g * 800 + s * 320), r0 = trread(L + wbR + g * 800 + s * 320);
        u32x2 a1 = u32x2{0u, 0u}, r1 = u32x2{0u, 0u};
        if (s < 2) { a1 = trread(L + wbA + g * 800 + s * 320 + 160); r1 = trread(L + wbR + g * 800 + s * 320 + 160); }
        oa = mfma32(u32x4{a0[0], a0[1], a1[0], a1[1]}, bq, oa);
        orr = mfma32(u32x4{r0[0], r0[1], r1[0], r1[1]}, bq, orr);
      }
      float pv[20];
#pragma unroll
      for (int f = 0; f < 16; ++f) pv[f] = oa[f];
#pragma unroll
      for (int f = 0; f < 4; ++f) pv[16 + f] = orr[f];
      float mx = pv[0], mn = pv[0];
#pragma unroll
      for (int f = 1; f < 20; ++f) { mx = fmaxf(mx, pv[f]); mn = fminf(mn, pv[f]); }
      const float bx = -mx * c1, bn = mn * c1;
      float ep[20], en[20], sp = 0.f, sn = 0.f;
#pragma unroll
      for (int f = 0; f < 20; ++f) {
        ep[f] = __builtin_amdgcn_exp2f(fmaf(pv[f], c1, bx));
        en[f] = __builtin_amdgcn_exp2f(fmaf(pv[f], -c1, bn));
        sp += ep[f]; sn += en[f];
      }
      const float rp = __builtin_amdgcn_rcpf(sp), rn = __builtin_amdgcn_rcpf(sn);
      unsigned o[20];
#pragma unroll
      for (int f = 0; f < 10; ++f) {
        o[f] = pk2(ep[2 * f] * rp, ep[2 * f + 1] * rp);
        o[10 + f] = pk2(en[2 * f] * rn, en[2 * f + 1] * rn);
      }
#pragma unroll
      for (int ci = 0; ci < 5; ++ci) out[g][ci] = u32x4{o[4 * ci], o[4 * ci + 1], o[4 * ci + 2], o[4 * ci + 3]};
    }
  };

  if (bt >= 2) {
    epi_M(IC<0>{}, mA);
    epi_M(IC<1>{}, mB);
  } else if (bt == 1) {
    u32x4 ph[4][5];
    epi_phi(ph);
    char* dst = ws + OFF_PQ + (size_t)(tg16 * 8 + 2 * ga + hh) * 5120 + r16 * 16;
#pragma unroll
    for (int g = 0; g < 4; ++g) {
#pragma unroll
      for (int ci = 0; ci < 4; ++ci) *(u32x4*)(dst + g * 1024 + ci * 256) = ph[g][ci];
      *(u32x4*)(dst + 4096 + g * 256) = ph[g][4];
    }
    epi_M(IC<1>{}, mB);
  } else {
    u32x4 ph[4][5];
    epi_phi(ph);
    const int trow = 32 * w + r;
#pragma unroll
    for (int g = 0; g < 4; ++g)
#pragma unroll
      for (int ci = 0; ci < 5; ++ci) *(LAS u32x4*)(L + L_PK + trow * PK_PITCH + (4 * hh + g) * 80 + ci * 16) = ph[g][ci];
#pragma unroll
    for (int g = 0; g < 4; ++g)
#pragma unroll
      for (int t = 0; t < 5; ++t) {
        const f32x4 c4 = csq(1, t, g);
        float v[4];
#pragma unroll
        for (int c = 0; c < 4; ++c) v[c] = fmaf(nmean, c4[c], acc[1][t][4 * g + c]) * rstd;
        *(LAS u32x2*)(L + L_V + trow * V_PITCH + (4 * hh + g) * 40 + 8 * t) = u32x2{pk2(v[0], v[1]), pk2(v[2], v[3])};
      }
    if (hh == 0) *(LAS u32x2*)(L + L_V + trow * V_PITCH + 320) = u32x2{0x00003F80u, 0u};
    __syncthreads();
#pragma unroll
    for (int ee = 0; ee < 2; ++ee) {
      const int e = 2 * w + ee;
      const int rowoff = 8 * hh + q;
      LAS char* pa0 = L + L_PK + rowoff * PK_PITCH + e * 80 + 32 * grp + 8 * p;
      LAS char* pa1 = L + L_PK + rowoff * PK_PITCH + e * 80 + 64 + 8 * (p & 1);
      LAS char* pb = L + L_V + rowoff * V_PITCH + (grp == 0 ? e * 40 + 8 * p : (p == 0 ? e * 40 + 32 : 320));
      f32x16 d0, d1;
#pragma unroll
      for (int i = 0; i < 16; ++i) { d0[i] = 0.f; d1[i] = 0.f; }
#pragma unroll
      for (int ks = 0; ks < 8; ++ks) {
        u32x2 a0 = trread(pa0 + ks * 16 * PK_PITCH), a1 = trread(pa0 + (ks * 16 + 4) * PK_PITCH);
        u32x2 c0 = trread(pa1 + ks * 16 * PK_PITCH), c1 = trread(pa1 + (ks * 16 + 4) * PK_PITCH);
        u32x2 b0 = trread(pb + ks * 16 * V_PITCH), b1 = trread(pb + (ks * 16 + 4) * V_PITCH);
        const u32x4 bb = u32x4{b0[0], b0[1], b1[0], b1[1]};
        d0 = mfma32(u32x4{a0[0], a0[1], a1[0], a1[1]}, bb, d0);
        d1 = mfma32(u32x4{c0[0], c0[1], c1[0], c1[1]}, bb, d1);
      }
      if (r <= 20) {
        float* dst = (float*)(ws + OFF_KVP) + ((size_t)(tg * 32 + 8 * ga + e) * 21 + r) * 40 + 4 * hh;
#pragma unroll
        for (int g = 0; g < 4; ++g) *(f32x4*)(dst + 8 * g) = f32x4{d0[4 * g], d0[4 * g + 1], d0[4 * g + 2], d0[4 * g + 3]};
        *(f32x4*)(dst + 32) = f32x4{d1[0], d1[1], d1[2], d1[3]};
      }
    }
  }
  asm volatile("" ::"v"(cpf));
  STAMP(0, 8);
  STAMP_FLUSH(0);
}

constexpr int K2_LDS = 163840;
__global__ __launch_bounds__(512, 2) void k2_out(const __hip_bfloat16* __restrict__ x_, char* __restrict__ ws,
                                                 __hip_bfloat16* __restrict__ out_) {
  extern __shared__ __attribute__((aligned(16))) char smem[];
  LAS char* L = (LAS char*)smem;
  const int tid = threadIdx.x, l = tid & 63;
  const int w = __builtin_amdgcn_readfirstlane(tid >> 6);
  const int b = blockIdx.x, xcd = b & 7, idx = b >> 3;
  const unsigned cpf = code_prefetch<5>(idx, tid);
  const int tg = xcd * 4 + (idx & 3);
  const int fg = idx >> 2;
  STAMP_DECL;
  STAMP(1, 0);
  const __amdgpu_buffer_rsrc_t rs = __builtin_amdgcn_make_buffer_rsrc((void*)ws, 0, (int)WS_BYTES, 0x00020000);
  const GAS u32x4* wt = (const GAS u32x4*)(ws + OFF_WT + (size_t)fg * 68 * 5120) + l;
  f32x4 acc[5][4];
#pragma unroll
  for (int nt = 0; nt < 5; ++nt)
#pragma unroll
    for (int i = 0; i < 4; ++i) acc[nt][i] = f32x4{0.f, 0.f, 0.f, 0.f};

  if (w < 4) {
    const GAS u32x4* hb = (const GAS u32x4*)(ws + OFF_H + (size_t)(tg * 4) * 49152 + (size_t)(12 * w) * 1024) + l;
    const GAS u32x4* wa = wt + (size_t)(12 * w) * 320;
    constexpr int WIN = 4;
    u32x4 fa[WIN][5], fb[WIN][4];
    auto ld = [&](auto K) {
      constexpr int k = decltype(K)::value;
#pragma unroll
      for (int i = 0; i < 4; ++i) fb[k % WIN][i] = hb[i * 3072 + k * 64];
#pragma unroll
      for (int nt = 0; nt < 5; ++nt) fa[k % WIN][nt] = wa[k * 320 + nt * 64];
    };
    sfor<0, WIN>(ld);
    __builtin_amdgcn_sched_barrier(0);
    sfor<0, 12>([&](auto K) {
      constexpr int k = decltype(K)::value;
      u32x4 ca[5], cb[4];
#pragma unroll
      for (int nt = 0; nt < 5; ++nt) ca[nt] = fa[k % WIN][nt];
#pragma unroll
      for (int i = 0; i < 4; ++i) cb[i] = fb[k % WIN][i];
#pragma unroll
      for (int nt = 0; nt < 5; ++nt)
#pragma unroll
        for (int i = 0; i < 4; ++i) acc[nt][i] = mfma16(ca[nt], cb[i], acc[nt][i]);
      __builtin_amdgcn_sched_barrier(0);
      if constexpr (k + WIN < 12) ld(IC<k + WIN>{});
      __builtin_amdgcn_sched_barrier(0);
    });
  } else {
    const int G = w - 4;
    if (w == 4 && b < 210) {
      const GAS f32x2* pp = (const GAS f32x2*)(ws + OFF_KVP) + b * 64 + l;
      f32x2 v[16];
#pragma unroll
      for (int k = 0; k < 16; ++k) v[k] = pp[k * 13440];
      f32x2 s = v[0];
#pragma unroll
      for (int k = 1; k < 16; ++k) s += v[k];
      __builtin_amdgcn_raw_buffer_store_b32(pk2(s[0], s[1]), rs, (int)OFF_KVT + b * 256 + l * 4, 0, 16);
      asm volatile("s_waitcnt vmcnt(0)" ::: "memory");
      if (l == 0) __hip_atomic_store((GAS unsigned*)(ws + OFF_FLAG) + b, 1u, __ATOMIC_RELAXED, __HIP_MEMORY_SCOPE_AGENT);
    }
    STAMP(1, 1);
    const GAS u32x4* pq = (const GAS u32x4*)(ws + OFF_PQ + (size_t)(tg * 4 * 8 + 2 * G) * 5120) + l;
    u32x4 pf[2][4][5];
#pragma unroll
    for (int u = 0; u < 2; ++u)
#pragma unroll
      for (int i = 0; i < 4; ++i)
#pragma unroll
        for (int f = 0; f < 5; ++f)
          if (!(u == 1 && (f == 2 || f == 3))) pf[u][i][f] = pq[i * 2560 + u * 320 + f * 64];
    __builtin_amdgcn_sched_barrier(0);
    {
      GAS unsigned* fl = (GAS unsigned*)(ws + OFF_FLAG);
      for (unsigned spin = 0; spin < (1u << 20); ++spin) {
        bool ok = true;
#pragma unroll
        for (int k = 0; k < 4; ++k) {
          const int id = k * 64 + l;
          const unsigned v = __hip_atomic_load(fl + (id < 210 ? id : 0), __ATOMIC_RELAXED, __HIP_MEMORY_SCOPE_AGENT);
          ok = ok && (v == 1u);
        }
        if (__all(ok)) break;
        __builtin_amdgcn_s_sleep(2);
      }
      __builtin_amdgcn_fence(__ATOMIC_ACQUIRE, "wavefront");
    }
    STAMP(1, 2);
    const int r = l & 15, g = l >> 4;
    const int row1 = (r & 3) == 0 ? 16 + (r >> 2) : 20;
    const GAS u32x4* wa = wt + (size_t)(48 + 5 * G) * 320;
    u32x4 ka[2][2][4];
    unsigned op[4][20];
    u32x4 ca[5][5];
    auto ldk = [&](auto HP) {
      constexpr int hp = decltype(HP)::value;
#pragma unroll
      for (int z = 0; z < 2; ++z) {
        const int hd = (2 * hp + z) & 3;
        const int hb = (int)OFF_KVT + (8 * G + 2 * hp + z) * 1680;
        ka[hp & 1][z][0] = __builtin_amdgcn_raw_buffer_load_b128(rs, hb + r * 80 + g * 16, 0, 16);
        ka[hp & 1][z][1] = __builtin_amdgcn_raw_buffer_load_b128(rs, hb + row1 * 80 + g * 16, 0, 16);
        ka[hp & 1][z][2] = __builtin_amdgcn_raw_buffer_load_b128(rs, g == hd ? hb + r * 80 + 64 : 0x7ffffff0, 0, 16);
        ka[hp & 1][z][3] = __builtin_amdgcn_raw_buffer_load_b128(rs, g == hd ? hb + row1 * 80 + 64 : 0x7ffffff0, 0, 16);
      }
    };
    auto ldw = [&](auto S) {
      constexpr int s = decltype(S)::value;
#pragma unroll
      for (int nt = 0; nt < 5; ++nt) ca[s][nt] = wa[s * 320 + nt * 64];
    };
    ldk(IC<0>{});
    ldk(IC<1>{});
    __builtin_amdgcn_sched_barrier(0);
    sfor<0, 4>([&](auto HP) {
      constexpr int hp = decltype(HP)::value;
      constexpr int u = hp >> 1;
#pragma unroll
      for (int i = 0; i < 4; ++i) {
        float ov[10];
#pragma unroll
        for (int z = 0; z < 2; ++z) {
          const int hd = (2 * hp + z) & 3;
          f32x4 o0 = f32x4{0.f, 0.f, 0.f, 0.f}, o1 = f32x4{0.f, 0.f, 0.f, 0.f};
          o0 = mfma16(ka[hp & 1][z][0], pf[u][i][hd], o0);
          o1 = mfma16(ka[hp & 1][z][1], pf[u][i][hd], o1);
          o0 = mfma16(ka[hp & 1][z][2], pf[u][i][4], o0);
          o1 = mfma16(ka[hp & 1][z][3], pf[u][i][4], o1);
          const float inv = __builtin_amdgcn_rcpf(o1[1] + 1e-6f);
#pragma unroll
          for (int v = 0; v < 4; ++v) ov[5 * z + v] = o0[v] * inv;
          ov[5 * z + 4] = o1[0] * inv;
        }
#pragma unroll
        for (int j = 0; j < 5; ++j) op[i][5 * hp + j] = pk2(ov[2 * j], ov[2 * j + 1]);
      }
      __builtin_amdgcn_sched_barrier(0);
      if constexpr (hp + 2 < 4) ldk(IC<hp + 2>{});
      if constexpr (hp == 0) {
#pragma unroll
        for (int i = 0; i < 4; ++i) { pf[1][i][2] = pq[i * 2560 + 320 + 128]; pf[1][i][3] = pq[i * 2560 + 320 + 192]; }
      }
      if constexpr (hp == 1) { ldw(IC<0>{}); ldw(IC<1>{}); }
      if constexpr (hp == 2) { ldw(IC<2>{}); ldw(IC<3>{}); }
      if constexpr (hp == 3) { ldw(IC<4>{}); }
      __builtin_amdgcn_sched_barrier(0);
    });
    STAMP(1, 3);
#pragma unroll
    for (int s = 0; s < 5; ++s) {
#pragma unroll
      for (int i = 0; i < 4; ++i) {
        const u32x4 ob = u32x4{op[i][4 * s], op[i][4 * s + 1], op[i][4 * s + 2], op[i][4 * s + 3]};
#pragma unroll
        for (int nt = 0; nt < 5; ++nt) acc[nt][i] = mfma16(ca[s][nt], ob, acc[nt][i]);
      }
    }
  }
  STAMP(1, 4);
#pragma unroll
  for (int nt = 0; nt < 5; ++nt)
#pragma unroll
    for (int i = 0; i < 4; ++i) *(LAS f32x4*)(L + ((w * 20 + nt * 4 + i) * 64 + l) * 16) = acc[nt][i];
  __syncthreads();
  STAMP(1, 5);
#pragma unroll
  for (int k = 0; k < 3; ++k) {
    const int t = w + 8 * k;
    if (t < 20) {
      f32x4 s = *(LAS f32x4*)(L + (t * 64 + l) * 16);
#pragma unroll
      for (int ww = 1; ww < 8; ++ww) s += *(LAS f32x4*)(L + ((ww * 20 + t) * 64 + l) * 16);
      const int nt = t >> 2, i = t & 3;
      const size_t off = ((size_t)(tg * 64 + i * 16 + (l & 15)) * 640 + fg * 80 + nt * 16 + 4 * (l >> 4)) * 2;
      const u32x2 xv = *(const u32x2*)((const char*)x_ + off);
      const float x0 = __builtin_bit_cast(float, xv[0] << 16), x1 = __builtin_bit_cast(float, xv[0] & 0xffff0000u);
      const float x2 = __builtin_bit_cast(float, xv[1] << 16), x3 = __builtin_bit_cast(float, xv[1] & 0xffff0000u);
      *(u32x2*)((char*)out_ + off) = u32x2{pk2(s[0] + x0, s[1] + x1), pk2(s[2] + x2, s[3] + x3)};
    }
  }
  asm volatile("" ::"v"(cpf));
  STAMP(1, 6);
  STAMP_FLUSH(1);
}

extern "C" void kernel_launch(void* const* d_in, const int* in_sizes, int n_in, void* d_out, int out_size, void* d_ws,
                              size_t ws_size, hipStream_t stream) {
  const __hip_bfloat16* x = (const __hip_bfloat16*)d_in[0];
  const __hip_bfloat16* wqkv = (const __hip_bfloat16*)d_in[1];
  const __hip_bfloat16* wphi = (const __hip_bfloat16*)d_in[2];
  const __hip_bfloat16* wo = (const __hip_bfloat16*)d_in[3];
  const __hip_bfloat16* wg = (const __hip_bfloat16*)d_in[5];
  const __hip_bfloat16* wu = (const __hip_bfloat16*)d_in[6];
  const __hip_bfloat16* wd = (const __hip_bfloat16*)d_in[7];
  (void)hipFuncSetAttribute((const void*)k1_proj, hipFuncAttributeMaxDynamicSharedMemorySize, K1_LDS);
  (void)hipFuncSetAttribute((const void*)k2_out, hipFuncAttributeMaxDynamicSharedMemorySize, K2_LDS);
  k1_proj<<<256, 256, K1_LDS, stream>>>(x, wqkv, wphi, wo, wg, wu, wd, (char*)d_ws);
  k2_out<<<256, 512, K2_LDS, stream>>>(x, (char*)d_ws, (__hip_bfloat16*)d_out);
}
```

```cpp
#define SC1ST
#include <hip/hip_runtime.h>
#include <hip/hip_bf16.h>
#include <stdint.h>

#define GAS __attribute__((address_space(1)))
#define LAS __attribute__((address_space(3)))
#define DI __device__ __forceinline__
typedef __bf16 bf16x8 __attribute__((ext_vector_type(8)));
typedef __bf16 bf16x2 __attribute__((ext_vector_type(2)));
typedef short s16x4 __attribute__((ext_vector_type(4)));
typedef float f32x4 __attribute__((ext_vector_type(4)));
typedef float f32x2 __attribute__((ext_vector_type(2)));
typedef unsigned u32x4 __attribute__((ext_vector_type(4)));
typedef unsigned u32x2 __attribute__((ext_vector_type(2)));

template <int N> struct IC { static constexpr int value = N; };
template <int B, int E, class F> DI void sfor(F&& f) {
  if constexpr (B < E) { f(IC<B>{}); sfor<B + 1, E>(f); }
}

constexpr unsigned OFF_H = 0;
constexpr unsigned OFF_PQ = 6291456;
constexpr unsigned OFF_WT = 11534336;
constexpr unsigned OFF_KVP = 14483456;
constexpr unsigned OFF_KVT = 15343616;
constexpr unsigned WS_BYTES = 15397376;
constexpr int NP = 8;

constexpr int NS = 5;
constexpr int SUB = 2688;
constexpr int WREG = SUB + 2560;
constexpr int XB = 16384;
constexpr int STG = XB + 2 * WREG;
constexpr int L_PT = 0;
constexpr int L_WPHI = L_PT + 16384;
constexpr int L_ZERO = L_WPHI + 4096;
constexpr int L_ST = L_ZERO + 1024;
constexpr int L_CSS = L_ST + 2048;
constexpr int L_RING = L_CSS + 2560;
constexpr int K1_LDS = L_RING + NS * STG;
constexpr int PK_PITCH = 336, V_PITCH = 176;
constexpr int L_PK = L_RING, L_V = L_PK + 256 * PK_PITCH;

DI unsigned pk2(float a, float b) {
  bf16x2 v; v[0] = (__bf16)a; v[1] = (__bf16)b;
  return __builtin_bit_cast(unsigned, v);
}
DI float dot2(unsigned a, unsigned b, float c) {
  return __builtin_amdgcn_fdot2_f32_bf16(__builtin_bit_cast(bf16x2, a), __builtin_bit_cast(bf16x2, b), c, false);
}
DI u32x2 trread(LAS char* p) {
  s16x4 v = __builtin_amdgcn_ds_read_tr16_b64_v4i16((LAS s16x4*)p);
  return __builtin_bit_cast(u32x2, v);
}
DI u32x4 trread2(LAS char* p, int hi) {
  u32x2 a = trread(p), b = trread(p + hi);
  return u32x4{a[0], a[1], b[0], b[1]};
}
DI f32x4 mfma16(u32x4 a, u32x4 b, f32x4 c) {
  return __builtin_amdgcn_mfma_f32_16x16x32_bf16(__builtin_bit_cast(bf16x8, a), __builtin_bit_cast(bf16x8, b), c, 0, 0, 0);
}
DI void mfma16a(f32x4& c, const u32x4& a, const u32x4& b) {
  asm volatile("v_mfma_f32_16x16x32_bf16 %0, %1, %2, %0" : "+v"(c) : "v"(a), "v"(b));
}
DI void dma16(const void* g, int ldsaddr) {
  asm volatile("s_mov_b32 m0, %1\n\ts_nop 0\n\tglobal_load_lds_dwordx4 %0, off" ::"v"(g), "s"(ldsaddr) : "memory");
}
template <int OFF = 0> DI void dma16s(unsigned voff, const char* sbase, int ldsaddr) {
  asm volatile("s_mov_b32 m0, %2\n\ts_nop 0\n\tglobal_load_lds_dwordx4 %0, %1 offset:%3" ::"v"(voff), "s"(sbase), "s"(ldsaddr - OFF), "i"(OFF) : "memory");
}
DI void dma16s_q(unsigned voff, const char* sbase, int ldsaddr) {
  asm volatile("s_mov_b32 m0, %2\n\ts_mov_b64 exec, 0xffff\n\tglobal_load_lds_dwordx4 %0, %1\n\ts_mov_b64 exec, -1" ::"v"(voff), "s"(sbase),
               "s"(ldsaddr)
               : "memory");
}
template <int C, int OFF = 0> DI void dma16s_if(int sel, unsigned voff, const char* sbase, int ldsaddr) {
  asm volatile("s_cmp_lg_u32 %3, %4\n\ts_cbranch_scc1 .Lskip%=\n\ts_mov_b32 m0, %2\n\ts_nop 0\n\tglobal_load_lds_dwordx4 %0, %1 offset:%5\n.Lskip%=:" ::"v"(voff),
               "s"(sbase), "s"(ldsaddr - OFF), "s"(sel), "i"(C), "i"(OFF)
               : "memory", "scc");
}
template <int C> DI void dma16s_q_if(int sel, unsigned voff, const char* sbase, int ldsaddr) {
  asm volatile("s_cmp_lg_u32 %3, %4\n\ts_cbranch_scc1 .Lskip%=\n\ts_mov_b32 m0, %2\n\ts_mov_b64 exec, 0xffff\n\tglobal_load_lds_dwordx4 %0, %1\n\ts_mov_b64 exec, -1\n.Lskip%=:" ::"v"(voff),
               "s"(sbase), "s"(ldsaddr), "s"(sel), "i"(C)
               : "memory", "scc");
}
DI int opaque(int v) { asm volatile("" : "+v"(v)); return v; }
DI void keep3(unsigned (&v)[3]) { asm volatile("" ::"v"(v[0]), "v"(v[1]), "v"(v[2])); }
DI void pin4(f32x4& v) { asm volatile("" : "+v"(v)); }
DI void pinu(u32x4& v) { asm volatile("" : "+v"(v)); }
DI void gst(char* p, u32x4 v) {
  asm volatile("global_store_dwordx4 %0, %1, off sc1\n\ts_nop 1" ::"v"(p), "v"(v) : "memory");
}
DI void gst(char* p, u32x2 v) {
  asm volatile("global_store_dwordx2 %0, %1, off sc1\n\ts_nop 1" ::"v"(p), "v"(v) : "memory");
}
DI void pinf(float& v) { asm volatile("" : "+v"(v)); }
#define WAIT_V(n) asm volatile("s_waitcnt vmcnt(%0)" ::"i"(n) : "memory")
#define STAMP_DECL
#define STAMP(k, i)
#define STAMP_FLUSH(k)
template <int NL> DI unsigned code_prefetch(int idx, int tid) {
  unsigned long long pc;
  asm volatile("s_getpc_b64 %0" : "=s"(pc));
  unsigned v = 0;
  if (tid < NL) v = *(const GAS unsigned*)(pc + (unsigned long long)(idx * NL + tid) * 64);
  return v;
}
DI const char* uptr(const char* p) {
  const unsigned long long v = (unsigned long long)p;
  const unsigned lo = __builtin_amdgcn_readfirstlane((unsigned)v), hi = __builtin_amdgcn_readfirstlane((unsigned)(v >> 32));
  return (const char*)(((unsigned long long)hi << 32) | lo);
}
#define BAR() __builtin_amdgcn_s_barrier()
#define SCHED() __builtin_amdgcn_sched_barrier(0)
#define K1_PF 15
#define K2_PF 3

constexpr int NPF = 0;
constexpr int xtra(int s) { return s == 0 ? 3 : (s == 7 ? 2 : 0); }
constexpr int grp(int j) { return j < 20 ? 4 : 0; }
constexpr int pend_at(int s) {
  int n = 0;
  if (s + 1 <= 4) {
    for (int j = s + 2; j <= 4; ++j) n += grp(j);
    n += NPF;
    for (int j = 0; j < s; ++j) n += grp(j + 5) + xtra(j);
  } else {
    n += xtra(s - 4);
    for (int j = s - 3; j < s; ++j) n += grp(j + 5) + xtra(j);
  }
  return n;
}

__global__ __launch_bounds__(512, 2) void k1_proj(const __hip_bfloat16* __restrict__ x_, const __hip_bfloat16* __restrict__ wqkv_,
                                                  const __hip_bfloat16* __restrict__ wphi_, const __hip_bfloat16* __restrict__ wo_,
                                                  const __hip_bfloat16* __restrict__ wg_, const __hip_bfloat16* __restrict__ wu_,
                                                  const __hip_bfloat16* __restrict__ wd_, char* __restrict__ ws) {
  extern __shared__ __attribute__((aligned(16))) char smem[];
  LAS char* L = (LAS char*)smem;
  const int lb = (int)(size_t)L;
  STAMP_DECL;
  STAMP(0, 0);
  int tid = threadIdx.x, l = tid & 63;
  const int w = __builtin_amdgcn_readfirstlane(tid >> 6);
  const int tq = w & 3, fgi = w >> 2;
  const int b = blockIdx.x, xcd = b & 7, idx = b >> 3;
  const unsigned cpf = code_prefetch<K1_PF>(idx, tid);
  const int tg = (xcd & 1) * 4 + (idx & 3);
  const int cg = (xcd >> 1) * 8 + (idx >> 2);
  const int ga = cg >> 2, bt = cg & 3;
  const int kind = bt == 0 ? 1 + fgi : ((bt == 1 && fgi == 0) ? 0 : 3);
  const int mj = bt == 1 ? ga : 8 + 4 * ga + 2 * (bt - 2) + fgi;
  const int mbase = min(40 * mj, 1496);

  unsigned xo[2];
#pragma unroll
  for (int i = 0; i < 2; ++i) {
    const int c = 128 * w + 64 * i + l, tok = c >> 2, slot = c & 3;
    const int swz = (0x78 >> (2 * ((tok >> 2) & 3))) & 3;
    xo[i] = tok * 1280 + 16 * (slot ^ swz);
  }
  const char* xs = uptr((const char*)x_ + (size_t)tg * 256 * 1280);
  const int wsub = tq >> 1;
  const int rs = kind == 3 ? 3072 : 3840;
  unsigned wo[2];
#pragma unroll
  for (int i = 0; i < 2; ++i) {
    const int c = min(64 * i + l, 79), pl = c / 5, ch = c - pl * 5;
    const int pos = 16 * (tq & 1) + pl;
    const int krow = (pos & 19) | ((pos & 4) << 1) | ((pos & 8) >> 1);
    wo[i] = krow * rs + 16 * ch;
  }
  const char* wsrc = uptr(kind == 3 ? (const char*)(wsub ? wu_ : wg_) + mbase * 2
                                    : (const char*)wqkv_ + (640 * kind + 80 * ga + 40 * wsub) * 2);
  const int winc = 32 * rs;
  const int xdst = lb + L_RING + w * 2048;
  const int wdst = lb + L_RING + XB + fgi * WREG + wsub * SUB + (tq & 1) * 1280;

  auto issue_op = [&](auto S, auto O) {
    constexpr int s = decltype(S)::value, o = decltype(O)::value;
    if constexpr (s < 20) {
      if constexpr (o < 2) dma16s<s * 64>(xo[o], xs, xdst + (s % NS) * STG + o * 1024);
      if constexpr (o == 2) dma16s(wo[0], wsrc, wdst + (s % NS) * STG);
      if constexpr (o == 3) { dma16s_q(wo[1], wsrc, wdst + (s % NS) * STG + 1024); wsrc += winc; }
    }
  };
  auto issue_stage = [&](auto S) { sfor<0, 4>([&](auto O) { issue_op(S, O); }); };
  issue_stage(IC<0>{});
  issue_stage(IC<1>{});
  issue_stage(IC<2>{});

  const int gw = b * 8 + w;
  const int fr1 = gw + 2048 < 2880 ? gw + 2048 : gw;
  const int wsel = w < 5 ? w : w - 5;
  auto side_dma = [&]() {
#pragma unroll
    for (int i = 0; i < 2; ++i) {
      const int fr = i ? fr1 : gw;
      const int fg = fr / 360, rem = fr - fg * 360, ks = rem / 5, nt = rem - ks * 5;
      const int slot = l >> 1, g_ = slot >> 3, j = slot & 7;
      const int u = (ks - 48) / 3, s_ = (ks - 48) - u * 3;
      const int id = min(8 * s_ + j, 19), e = id / 5, v = id - e * 5;
      const int d = v < 4 ? 4 * g_ + v : 16 + g_;
      const int row = ks < 48 ? 32 * ks + slot : (4 * u + e) * 20 + d;
      const char* pb_ = uptr(ks < 48 ? (const char*)wd_ : (const char*)wo_);
      dma16s(row * 1280 + (80 * fg + 16 * nt + 8 * (l & 1)) * 2, pb_, lb + L_PT + (w * 2 + i) * 1024);
    }
    dma16s(ga * 3200 + min((w & 3) * 64 + l, 199) * 16, (const char*)wphi_, lb + L_WPHI + (w & 3) * 1024);
  };

  int tok = l & 15, g = l >> 4, q = (l & 15) >> 2, p = l & 3;
  auto relane = [&]() { tid = opaque(threadIdx.x); l = tid & 63; tok = l & 15; g = l >> 4; q = (l & 15) >> 2; p = l & 3; };
  const int abase = L_RING + XB + fgi * WREG + (p >> 1) * SUB + (16 * (g >> 1) + 4 * (g & 1) + q) * 80 + 40 * (p & 1);
  int ab[2], cb[2], bb[2][2];
  {
    const int swz = (0x78 >> (2 * (tok >> 2))) & 3;
#pragma unroll
    for (int h = 0; h < 2; ++h) {
      ab[h] = opaque(abase + h * 2 * STG);
      cb[h] = opaque(abase + 8 * tq + h * 2 * STG);
#pragma unroll
      for (int i = 0; i < 2; ++i) bb[h][i] = opaque(L_RING + (64 * tq + 32 * (i ^ fgi) + tok) * 64 + 16 * (g ^ swz) + h * 2 * STG);
    }
  }

  f32x4 acc[5][4];
#pragma unroll
  for (int t = 0; t < 5; ++t)
#pragma unroll
    for (int i = 0; i < 4; ++i) acc[t][i] = f32x4{0.f, 0.f, 0.f, 0.f};
  f32x4 csa = f32x4{0.f, 0.f, 0.f, 0.f}, csb = f32x4{0.f, 0.f, 0.f, 0.f};
  f32x4 sxa[2] = {f32x4{0.f, 0.f, 0.f, 0.f}, f32x4{0.f, 0.f, 0.f, 0.f}};
  f32x4 gra[2] = {f32x4{0.f, 0.f, 0.f, 0.f}, f32x4{0.f, 0.f, 0.f, 0.f}};
  u32x4 ONES4 = u32x4{0x3F803F80u, 0x3F803F80u, 0x3F803F80u, 0x3F803F80u};
  asm volatile("" : "+v"(ONES4));
#pragma unroll
  for (int t = 0; t < 5; ++t)
#pragma unroll
    for (int i = 0; i < 4; ++i) pin4(acc[t][i]);
  pin4(csa); pin4(csb); pin4(sxa[0]); pin4(sxa[1]); pin4(gra[0]); pin4(gra[1]);
  u32x4 fc[2];
  auto readC = [&](auto S) {
    constexpr int s = decltype(S)::value;
    constexpr int sl = s % NS, h = sl >= 2 ? 1 : 0;
    fc[s & 1] = trread2(L + cb[h] + (sl - 2 * h) * STG, 640);
  };

  u32x4 fa[2][5], fb[2][4];
  auto readA = [&](auto S, auto T) {
    constexpr int s = decltype(S)::value, t = decltype(T)::value;
    constexpr int sl = s % NS, h = sl >= 2 ? 1 : 0;
    fa[s & 1][t] = trread2(L + ab[h] + (sl - 2 * h) * STG + 8 * t, 640);
  };
  auto readB = [&](auto S, auto I) {
    constexpr int s = decltype(S)::value, i = decltype(I)::value;
    constexpr int sl = s % NS, h = sl >= 2 ? 1 : 0;
    fb[s & 1][i] = *(LAS u32x4*)(L + bb[h][i >> 1] + (sl - 2 * h) * STG + (i & 1) * 1024);
  };

  STAMP(0, 1);
  WAIT_V(8);
  BAR();
  STAMP(0, 2);
  sfor<0, 4>([&](auto I) { readB(IC<0>{}, I); });
  sfor<0, 5>([&](auto T) { readA(IC<0>{}, T); });
  readC(IC<0>{});
  issue_stage(IC<3>{});
  issue_stage(IC<4>{});
  if (tid < 64) *(LAS u32x4*)(L + L_ZERO + tid * 16) = u32x4{0, 0, 0, 0};
  SCHED();

  sfor<0, 20>([&](auto S) {
    constexpr int s = decltype(S)::value;
    if constexpr (s + 1 < 20) {
      constexpr int pend = pend_at(s);
      asm volatile("s_waitcnt vmcnt(%0) lgkmcnt(0)" ::"i"(pend) : "memory");
      BAR();
    }
    sfor<0, 5>([&](auto T) {
      constexpr int t = decltype(T)::value;
      sfor<0, 4>([&](auto I) {
        constexpr int i = decltype(I)::value;
        mfma16a(acc[t][i], fa[s & 1][t], fb[s & 1][i]);
        if constexpr (s + 1 < 20) {
          if constexpr (t == 0) readB(IC<s + 1>{}, I);
          if constexpr (i == 1) readA(IC<s + 1>{}, T);
        }
        if constexpr (i == 3 && t < 4) issue_op(IC<s + NS>{}, T);
        if constexpr (t == 4 && i == 0 && s + 1 < 20) readC(IC<s + 1>{});
        if constexpr (t == 2 && i == 3) mfma16a(csa, fc[s & 1], ONES4);
        if constexpr (t == 1 && i == 3) mfma16a(sxa[0], ONES4, fb[s & 1][0]);
        if constexpr (t == 3 && i == 3) mfma16a(sxa[1], ONES4, fb[s & 1][1]);
        if constexpr (t == 0 && i == 3) mfma16a(gra[0], fb[s & 1][0], fb[s & 1][0]);
        if constexpr (t == 4 && i == 3) mfma16a(gra[1], fb[s & 1][1], fb[s & 1][1]);
        SCHED();
      });
    });
    if ((s & 3) == tq) mfma16a(csb, fa[s & 1][4], ONES4);
    if constexpr (s == 0) { relane(); side_dma(); }
    if constexpr (s == 7) {
      relane();
#pragma unroll
      for (int i = 0; i < 2; ++i) {
        const u32x4 v = trread2(L + L_PT + (w * 2 + i) * 1024 + (8 * g + q) * 32 + 8 * p, 128);
        gst(ws + OFF_WT + (size_t)(i ? fr1 : gw) * 1024 + l * 16, v);
      }
    }
    if constexpr (s == 4) STAMP(0, 3);
    if constexpr (s == 9) STAMP(0, 4);
    if constexpr (s == 14) STAMP(0, 5);
    SCHED();
  });
  asm volatile("s_nop 7\n\ts_nop 7" ::: "memory");
  SCHED();
  STAMP(0, 6);
  relane();
  if (b < 210 && tid < 64) *(unsigned*)(ws + OFF_KVT + b * 256 + tid * 4) = 0xFFFFFFFFu;

#pragma unroll
  for (int i = 0; i < 2; ++i) {
    const float c = p == 0 ? gra[i][0] : (p == 1 ? gra[i][1] : (p == 2 ? gra[i][2] : gra[i][3]));
    const float a = sxa[i][0];
    const float mean = a * (1.f / 640.f);
    const float var = fmaxf(c * (1.f / 640.f) - mean * mean, 0.f);
    if (g == q) *(LAS f32x2*)(L + L_ST + (64 * tq + 16 * ((i + 2 * fgi) & 3) + tok) * 8) = f32x2{-mean, var + 1e-5f};
  }
  if (tok == 0) {
    *(LAS f32x4*)(L + L_CSS + (fgi * 128 + tq * 16 + 4 * g) * 4) = csa;
    *(LAS f32x4*)(L + L_CSS + (fgi * 128 + 64 + tq * 16 + 4 * g) * 4) = csb;
  }
  __syncthreads();
  float nmean[4], vare[4], rstd[4];
#pragma unroll
  for (int i = 0; i < 4; ++i) {
    const f32x2 v = *(LAS f32x2*)(L + L_ST + (64 * tq + 16 * ((i + 2 * fgi) & 3) + tok) * 8);
    nmean[i] = v[0]; vare[i] = v[1];
    rstd[i] = __builtin_amdgcn_rsqf(v[1]);
  }
#pragma unroll
  for (int t = 0; t < 5; ++t) {
    f32x4 c4 = *(LAS f32x4*)(L + L_CSS + (fgi * 128 + t * 16 + 4 * g) * 4);
    if (t == 4) {
#pragma unroll
      for (int k = 1; k < 4; ++k) c4 += *(LAS f32x4*)(L + L_CSS + (fgi * 128 + 64 + k * 16 + 4 * g) * 4);
    }
#pragma unroll
    for (int i = 0; i < 4; ++i)
#pragma unroll
      for (int c = 0; c < 4; ++c) acc[t][i][c] = fmaf(nmean[i], c4[c], acc[t][i][c]);
  }
  STAMP(0, 7);
  const int tile0 = tg * 16 + tq * 4;

  if (kind == 3) {
    const int hi = l >> 5, pp = g & 1;
#pragma unroll
    for (int i = 0; i < 2; ++i) {
      const float rs_ = hi ? rstd[i + 2] : rstd[i], ve = hi ? vare[i + 2] : vare[i];
      const float c1 = -2.0f * 0.7978845608028654f * 1.4426950408889634f;
      const float cA = c1 * rs_, cB = c1 * 0.044715f * rs_ * rs_ * rs_;
      unsigned hp[10];
#pragma unroll
      for (int t = 0; t < 5; ++t) {
        float hv[4];
#pragma unroll
        for (int c = 0; c < 4; ++c) {
          const float va = acc[t][i][c], vb = acc[t][i + 2][c];
          const auto sw = __builtin_amdgcn_permlane32_swap(__float_as_uint(va), __float_as_uint(vb), false, false);
          const unsigned s0 = sw[0], s1 = sw[1];
          const float gg = __uint_as_float(s0), uu = __uint_as_float(s1);
          const float arg = fmaf(gg * gg, cB, cA) * gg;
          const float e = __builtin_amdgcn_exp2f(arg);
          const float rr = __builtin_amdgcn_rcpf(fmaf(e, ve, ve));
          hv[c] = gg * uu * rr;
        }
        hp[2 * t] = pk2(hv[0], hv[1]);
        hp[2 * t + 1] = pk2(hv[2], hv[3]);
      }
      const int tl = tile0 + ((i + 2 * hi + 2 * fgi) & 3);
      char* dst = ws + OFF_H + (size_t)tl * 49152 + (mbase >> 3) * 256 + tok * 16;
      if (pp == 0) {
        gst(dst, u32x4{hp[0], hp[1], hp[2], hp[3]});
        gst(dst + 256, u32x4{hp[4], hp[5], hp[6], hp[7]});
        gst(dst + 512, u32x2{hp[8], hp[9]});
      } else {
        gst(dst + 520, u32x2{hp[0], hp[1]});
        gst(dst + 768, u32x4{hp[2], hp[3], hp[4], hp[5]});
        gst(dst + 1024, u32x4{hp[6], hp[7], hp[8], hp[9]});
      }
    }
  } else if (kind == 2) {
#pragma unroll
    for (int i = 0; i < 4; ++i) {
      const int trow = 64 * tq + 16 * ((i + 2) & 3) + tok;
#pragma unroll
      for (int t = 0; t < 5; ++t)
        *(LAS u32x2*)(L + L_V + trow * V_PITCH + 40 * g + 8 * t) =
            u32x2{pk2(acc[t][i][0] * rstd[i], acc[t][i][1] * rstd[i]), pk2(acc[t][i][2] * rstd[i], acc[t][i][3] * rstd[i])};
      if (g == 0) *(LAS u32x2*)(L + L_V + trow * V_PITCH + 160) = u32x2{0x00003F80u, 0u};
    }
  } else {
    u32x4 bq[4][3];
#pragma unroll
    for (int i = 0; i < 4; ++i) {
#pragma unroll
      for (int s = 0; s < 2; ++s)
        bq[i][s] = u32x4{pk2(acc[2 * s][i][0], acc[2 * s][i][1]), pk2(acc[2 * s][i][2], acc[2 * s][i][3]),
                         pk2(acc[2 * s + 1][i][0], acc[2 * s + 1][i][1]), pk2(acc[2 * s + 1][i][2], acc[2 * s + 1][i][3])};
      bq[i][2] = u32x4{pk2(acc[4][i][0], acc[4][i][1]), pk2(acc[4][i][2], acc[4][i][3]), 0u, 0u};
    }
    const int wb = (p == g) ? (L_WPHI + g * 800 + q * 40) : L_ZERO;
    f32x4 pq[5][4];
#pragma unroll
    for (int tt = 0; tt < 5; ++tt) {
      const u32x4 a0 = trread2(L + wb + 8 * tt, 160), a1 = trread2(L + wb + 8 * tt + 320, 160);
      const u32x2 a2l = trread(L + wb + 8 * tt + 640);
      const u32x4 a2 = u32x4{a2l[0], a2l[1], 0u, 0u};
#pragma unroll
      for (int i = 0; i < 4; ++i) {
        f32x4 o = f32x4{0.f, 0.f, 0.f, 0.f};
        o = mfma16(a0, bq[i][0], o);
        o = mfma16(a1, bq[i][1], o);
        o = mfma16(a2, bq[i][2], o);
        pq[tt][i] = o;
      }
    }
#pragma unroll
    for (int i = 0; i < 4; ++i) {
      const float c1 = rstd[i] * 1.4426950408889634f;
      float mx = pq[0][i][0], mn = pq[0][i][0];
#pragma unroll
      for (int f = 1; f < 20; ++f) { mx = fmaxf(mx, pq[f >> 2][i][f & 3]); mn = fminf(mn, pq[f >> 2][i][f & 3]); }
      const float bx = -mx * c1, bn = mn * c1;
      float ep[20], en[20], sp = 0.f, sn = 0.f;
#pragma unroll
      for (int f = 0; f < 20; ++f) {
        ep[f] = __builtin_amdgcn_exp2f(fmaf(pq[f >> 2][i][f & 3], c1, bx));
        en[f] = __builtin_amdgcn_exp2f(fmaf(pq[f >> 2][i][f & 3], -c1, bn));
        sp += ep[f]; sn += en[f];
      }
      const float rp = __builtin_amdgcn_rcpf(sp), rn = __builtin_amdgcn_rcpf(sn);
      unsigned o[20];
#pragma unroll
      for (int f = 0; f < 10; ++f) {
        o[f] = pk2(ep[2 * f] * rp, ep[2 * f + 1] * rp);
        o[10 + f] = pk2(en[2 * f] * rn, en[2 * f + 1] * rn);
      }
      if (kind == 0) {
        char* dst = ws + OFF_PQ + (size_t)((tile0 + i) * 8 + ga) * 5120 + tok * 16;
#pragma unroll
        for (int ci = 0; ci < 4; ++ci) gst(dst + g * 1024 + ci * 256, u32x4{o[4 * ci], o[4 * ci + 1], o[4 * ci + 2], o[4 * ci + 3]});
        gst(dst + 4096 + g * 256, u32x4{o[16], o[17], o[18], o[19]});
      } else {
        const int trow = 64 * tq + 16 * i + tok;
#pragma unroll
        for (int ci = 0; ci < 5; ++ci)
          *(LAS u32x4*)(L + L_PK + trow * PK_PITCH + g * 80 + ci * 16) = u32x4{o[4 * ci], o[4 * ci + 1], o[4 * ci + 2], o[4 * ci + 3]};
      }
    }
  }
  if (bt == 0) {
    __syncthreads();
    const int e = w >> 1, bh = w & 1;
    const int rowoff = 8 * g + q;
    LAS char* pa = L + L_PK + rowoff * PK_PITCH + e * 80 + 8 * p;
    LAS char* pa2 = L + L_PK + rowoff * PK_PITCH + e * 80 + 64 + 8 * (p & 1);
    LAS char* pb = L + L_V + rowoff * V_PITCH + (bh == 0 ? e * 40 + 8 * p : (p == 0 ? e * 40 + 32 : 160));
    f32x4 d[3];
#pragma unroll
    for (int k = 0; k < 3; ++k) d[k] = f32x4{0.f, 0.f, 0.f, 0.f};
#pragma unroll
    for (int ks = 0; ks < 8; ++ks) {
      const u32x4 bb = trread2(pb + ks * 32 * V_PITCH, 4 * V_PITCH);
      d[0] = mfma16(trread2(pa + ks * 32 * PK_PITCH, 4 * PK_PITCH), bb, d[0]);
      d[1] = mfma16(trread2(pa + ks * 32 * PK_PITCH + 32, 4 * PK_PITCH), bb, d[1]);
      d[2] = mfma16(trread2(pa2 + ks * 32 * PK_PITCH, 4 * PK_PITCH), bb, d[2]);
    }
    const int dcol = 16 * bh + tok;
    if (dcol <= 20) {
      float* dst = (float*)(ws + OFF_KVP) + ((size_t)(tg * 32 + 4 * ga + e) * 21 + dcol) * 40 + 4 * g;
      *(f32x4*)(dst) = d[0];
      *(f32x4*)(dst + 16) = d[1];
      if (g < 2) *(f32x4*)(dst + 32) = d[2];
    }
  }
  asm volatile("" ::"v"(cpf));
  STAMP(0, 8);
  STAMP_FLUSH(0);
}

constexpr int K2_LDS = 163840;
__global__ __launch_bounds__(512, 2) void k2_out(const __hip_bfloat16* __restrict__ x_, char* __restrict__ ws,
                                                 __hip_bfloat16* __restrict__ out_) {
  extern __shared__ __attribute__((aligned(16))) char smem[];
  LAS char* L = (LAS char*)smem;
  const int tid = threadIdx.x, l = tid & 63;
  const int w = __builtin_amdgcn_readfirstlane(tid >> 6);
  const int b = blockIdx.x, xcd = b & 7, idx = b >> 3;
  STAMP_DECL;
  STAMP(1, 0);
  const __amdgpu_buffer_rsrc_t rs = __builtin_amdgcn_make_buffer_rsrc((void*)ws, 0, (int)WS_BYTES, 0x00020000);
  const bool red = (w == 0 && b < 210);
  f32x2 pv[NP];
  if (red) {
    const GAS f32x2* pp = (const GAS f32x2*)(ws + OFF_KVP) + b * 64 + l;
#pragma unroll
    for (int k = 0; k < NP; ++k) pv[k] = pp[k * 13440];
  }
  SCHED();
  BAR();
  const unsigned cpf = code_prefetch<K2_PF>(idx, tid);
  const int tg = xcd * 4 + (idx & 3);
  const int fg = idx >> 2;
  const int vo = l * 16;
  const int so_h = (int)OFF_H + tg * 4 * 49152 + 6 * w * 1024;
  const int so_a = (int)OFF_WT + fg * 72 * 5120 + 6 * w * 5120;
  const int so_p = (int)OFF_PQ + (tg * 32 + w) * 5120;
  const int so_o = (int)OFF_WT + fg * 72 * 5120 + (48 + 3 * w) * 5120;
  auto ldg = [&](int so) -> u32x4 { return __builtin_amdgcn_raw_buffer_load_b128(rs, vo, so, 0); };
  f32x4 acc[5][4];
#pragma unroll
  for (int nt = 0; nt < 5; ++nt)
#pragma unroll
    for (int i = 0; i < 4; ++i) { acc[nt][i] = f32x4{0.f, 0.f, 0.f, 0.f}; pin4(acc[nt][i]); }

  u32x4 fa[6][5], fb[6][4], pf[4][5], ca[3][5];
  auto ld = [&](auto K) {
    constexpr int k = decltype(K)::value;
#pragma unroll
    for (int i = 0; i < 4; ++i) fb[k][i] = ldg(so_h + i * 49152 + k * 1024);
#pragma unroll
    for (int nt = 0; nt < 5; ++nt) fa[k][nt] = ldg(so_a + k * 5120 + nt * 1024);
    SCHED();
  };
  auto mm = [&](auto K) {
    constexpr int k = decltype(K)::value;
#pragma unroll
    for (int nt = 0; nt < 5; ++nt)
#pragma unroll
      for (int i = 0; i < 4; ++i) mfma16a(acc[nt][i], fa[k][nt], fb[k][i]);
    SCHED();
  };
  auto ldp = [&](int hp) {
#pragma unroll
    for (int i = 0; i < 4; ++i) {
      pf[i][2 * hp] = ldg(so_p + i * 40960 + 2 * hp * 1024);
      pf[i][2 * hp + 1] = ldg(so_p + i * 40960 + (2 * hp + 1) * 1024);
      if (hp == 0) pf[i][4] = ldg(so_p + i * 40960 + 4096);
    }
    SCHED();
  };
  auto ldo = [&](int s) {
#pragma unroll
    for (int nt = 0; nt < 5; ++nt) ca[s][nt] = ldg(so_o + s * 5120 + nt * 1024);
    SCHED();
  };
  const int r = l & 15, g = l >> 4;
  const int row1 = (r & 3) == 0 ? 16 + (r >> 2) : 20;
  const int so_k = (int)OFF_KVT + 4 * w * 1680;
  const int ko0 = r * 80 + g * 16, ko1 = row1 * 80 + g * 16, kt0 = r * 80 + g * 1680 + 64, kt1 = row1 * 80 + g * 1680 + 64;
  u32x4 ka[4][2], kt[2];
  auto ldk = [&](int hp) {
#pragma unroll
    for (int hd = 2 * hp; hd < 2 * hp + 2; ++hd) {
      ka[hd][0] = __builtin_amdgcn_raw_buffer_load_b128(rs, ko0, so_k + hd * 1680, 16);
      ka[hd][1] = __builtin_amdgcn_raw_buffer_load_b128(rs, ko1, so_k + hd * 1680, 16);
    }
    if (hp == 0) {
      kt[0] = __builtin_amdgcn_raw_buffer_load_b128(rs, kt0, so_k, 16);
      kt[1] = __builtin_amdgcn_raw_buffer_load_b128(rs, kt1, so_k, 16);
    }
  };
  auto kbad = [&](int hp) -> bool {
    unsigned mxw = 0u;
#pragma unroll
    for (int k = 0; k < 2; ++k)
#pragma unroll
      for (int j = 0; j < 4; ++j) {
        if (hp == 0) mxw = max(mxw, kt[k][j]);
#pragma unroll
        for (int hd = 2 * hp; hd < 2 * hp + 2; ++hd) mxw = max(mxw, ka[hd][k][j]);
      }
    return __any(mxw == 0xFFFFFFFFu);
  };

  if (red) {
    f32x2 s = pv[0];
#pragma unroll
    for (int k = 1; k < NP; ++k) s += pv[k];
    __builtin_amdgcn_raw_buffer_store_b32(pk2(s[0], s[1]), rs, (int)OFF_KVT + b * 256 + l * 4, 0, 16);
  }
  SCHED();
  STAMP(1, 1);
  ld(IC<0>{}); ld(IC<1>{}); ld(IC<2>{});
  mm(IC<0>{}); ld(IC<3>{});
  asm volatile("" ::"v"(cpf));
  mm(IC<1>{}); ldp(0);
  mm(IC<2>{}); ldk(0); SCHED();
  mm(IC<3>{}); ldk(1); ldp(1);
  STAMP(1, 2);
  unsigned op[4][12];
#pragma unroll
  for (int hp = 0; hp < 2; ++hp) {
    if (__builtin_expect(kbad(hp), 0)) {
      unsigned spin = 0;
      do {
        __builtin_amdgcn_s_sleep(4);
        ldk(hp);
      } while (kbad(hp) && ++spin < (1u << 14));
    }
    if (hp == 0) STAMP(1, 3);
    u32x4 km[2][2];
#pragma unroll
    for (int z = 0; z < 2; ++z)
#pragma unroll
      for (int k = 0; k < 2; ++k) km[z][k] = (g == 2 * hp + z) ? kt[k] : u32x4{0u, 0u, 0u, 0u};
#pragma unroll
    for (int i = 0; i < 4; ++i) {
      float ov[10];
#pragma unroll
      for (int z = 0; z < 2; ++z) {
        const int hd = 2 * hp + z;
        f32x4 o0 = f32x4{0.f, 0.f, 0.f, 0.f}, o1 = f32x4{0.f, 0.f, 0.f, 0.f};
        o0 = mfma16(ka[hd][0], pf[i][hd], o0);
        o1 = mfma16(ka[hd][1], pf[i][hd], o1);
        o0 = mfma16(km[z][0], pf[i][4], o0);
        o1 = mfma16(km[z][1], pf[i][4], o1);
        const float inv = __builtin_amdgcn_rcpf(o1[1] + 1e-6f);
#pragma unroll
        for (int v = 0; v < 4; ++v) ov[5 * z + v] = o0[v] * inv;
        ov[5 * z + 4] = o1[0] * inv;
      }
#pragma unroll
      for (int j = 0; j < 5; ++j) op[i][5 * hp + j] = (unsigned)opaque((int)pk2(ov[2 * j], ov[2 * j + 1]));
      op[i][10] = 0u; op[i][11] = 0u;
      if (i & 1) SCHED();
    }
    SCHED();
    if (hp == 0) ld(IC<4>{});
    else { ld(IC<5>{}); ldo(0); }
  }
  u32x4 ob[3][4];
#pragma unroll
  for (int s = 0; s < 3; ++s)
#pragma unroll
    for (int i = 0; i < 4; ++i) { ob[s][i] = u32x4{op[i][4 * s], op[i][4 * s + 1], op[i][4 * s + 2], op[i][4 * s + 3]}; pinu(ob[s][i]); }
  SCHED();
  mm(IC<4>{}); ldo(1); ldo(2);
  u32x2 xv[3];
#pragma unroll
  for (int k = 0; k < 3; ++k) {
    const int t = min(w + 8 * k, 19), nt = t >> 2, i = t & 3;
    xv[k] = *(const u32x2*)((const char*)x_ + ((size_t)(tg * 64 + i * 16 + (l & 15)) * 640 + fg * 80 + nt * 16 + 4 * (l >> 4)) * 2);
  }
  SCHED();
  mm(IC<5>{});
#pragma unroll
  for (int s = 0; s < 3; ++s) {
#pragma unroll
    for (int nt = 0; nt < 5; ++nt)
#pragma unroll
      for (int i = 0; i < 4; ++i) mfma16a(acc[nt][i], ca[s][nt], ob[s][i]);
  }
  SCHED();
  asm volatile("s_nop 7\n\ts_nop 7" ::: "memory");
  SCHED();
  STAMP(1, 4);
#pragma unroll
  for (int nt = 0; nt < 5; ++nt)
#pragma unroll
    for (int i = 0; i < 4; ++i) *(LAS f32x4*)(L + ((w * 20 + nt * 4 + i) * 64 + l) * 16) = acc[nt][i];
  __syncthreads();
  STAMP(1, 5);
#pragma unroll
  for (int k = 0; k < 3; ++k) {
    const int t = w + 8 * k;
    if (t < 20) {
      f32x4 s = *(LAS f32x4*)(L + (t * 64 + l) * 16);
#pragma unroll
      for (int ww = 1; ww < 8; ++ww) s += *(LAS f32x4*)(L + ((ww * 20 + t) * 64 + l) * 16);
      const int nt = t >> 2, i = t & 3;
      const size_t off = ((size_t)(tg * 64 + i * 16 + (l & 15)) * 640 + fg * 80 + nt * 16 + 4 * (l >> 4)) * 2;
      const float x0 = __builtin_bit_cast(float, xv[k][0] << 16), x1 = __builtin_bit_cast(float, xv[k][0] & 0xffff0000u);
      const float x2 = __builtin_bit_cast(float, xv[k][1] << 16), x3 = __builtin_bit_cast(float, xv[k][1] & 0xffff0000u);
      *(u32x2*)((char*)out_ + off) = u32x2{pk2(s[0] + x0, s[1] + x1), pk2(s[2] + x2, s[3] + x3)};
    }
  }
  STAMP(1, 6);
  STAMP_FLUSH(1);
}

extern "C" void kernel_launch(void* const* d_in, const int* in_sizes, int n_in, void* d_out, int out_size, void* d_ws,
                              size_t ws_size, hipStream_t stream) {
  const __hip_bfloat16* x = (const __hip_bfloat16*)d_in[0];
  const __hip_bfloat16* wqkv = (const __hip_bfloat16*)d_in[1];
  const __hip_bfloat16* wphi = (const __hip_bfloat16*)d_in[2];
  const __hip_bfloat16* wo = (const __hip_bfloat16*)d_in[3];
  const __hip_bfloat16* wg = (const __hip_bfloat16*)d_in[5];
  const __hip_bfloat16* wu = (const __hip_bfloat16*)d_in[6];
  const __hip_bfloat16* wd = (const __hip_bfloat16*)d_in[7];
  (void)hipFuncSetAttribute((const void*)k1_proj, hipFuncAttributeMaxDynamicSharedMemorySize, K1_LDS);
  (void)hipFuncSetAttribute((const void*)k2_out, hipFuncAttributeMaxDynamicSharedMemorySize, K2_LDS);
  k1_proj<<<256, 512, K1_LDS, stream>>>(x, wqkv, wphi, wo, wg, wu, wd, (char*)d_ws);
  k2_out<<<256, 512, K2_LDS, stream>>>(x, (char*)d_ws, (__hip_bfloat16*)d_out);
}
```

```cpp
#define SC1ST
#include <hip/hip_runtime.h>
#include <hip/hip_bf16.h>
#include <stdint.h>

#define GAS __attribute__((address_space(1)))
#define LAS __attribute__((address_space(3)))
#define DI __device__ __forceinline__
typedef __bf16 bf16x8 __attribute__((ext_vector_type(8)));
typedef __bf16 bf16x2 __attribute__((ext_vector_type(2)));
typedef short s16x4 __attribute__((ext_vector_type(4)));
typedef float f32x4 __attribute__((ext_vector_type(4)));
typedef float f32x2 __attribute__((ext_vector_type(2)));
typedef unsigned u32x4 __attribute__((ext_vector_type(4)));
typedef unsigned u32x2 __attribute__((ext_vector_type(2)));

template <int N> struct IC { static constexpr int value = N; };
template <int B, int E, class F> DI void sfor(F&& f) {
  if constexpr (B < E) { f(IC<B>{}); sfor<B + 1, E>(f); }
}

constexpr unsigned OFF_H = 0;
constexpr unsigned OFF_PQ = 6291456;
constexpr unsigned OFF_WT = 11534336;
constexpr unsigned OFF_KVP = 14483456;
constexpr unsigned OFF_KVT = 15343616;
constexpr unsigned WS_BYTES = 15397376;
constexpr int NP = 8;

constexpr int NS = 5;
constexpr int SUB = 2688;
constexpr int WREG = SUB + 2560;
constexpr int XB = 16384;
constexpr int STG = XB + 2 * WREG;
constexpr int L_PT = 0;
constexpr int L_WPHI = L_PT + 16384;
constexpr int L_ZERO = L_WPHI + 4096;
constexpr int L_ST = L_ZERO + 1024;
constexpr int L_CSS = L_ST + 2048;
constexpr int L_DUMMY = L_CSS + 2048;
constexpr int L_RING = L_CSS + 2560;
constexpr int K1_LDS = L_RING + NS * STG;
constexpr int PK_PITCH = 336, V_PITCH = 176;
constexpr int L_PK = L_RING, L_V = L_PK + 256 * PK_PITCH;

DI unsigned pk2(float a, float b) {
  bf16x2 v; v[0] = (__bf16)a; v[1] = (__bf16)b;
  return __builtin_bit_cast(unsigned, v);
}
DI float dot2(unsigned a, unsigned b, float c) {
  return __builtin_amdgcn_fdot2_f32_bf16(__builtin_bit_cast(bf16x2, a), __builtin_bit_cast(bf16x2, b), c, false);
}
DI u32x2 trread(LAS char* p) {
  s16x4 v = __builtin_amdgcn_ds_read_tr16_b64_v4i16((LAS s16x4*)p);
  return __builtin_bit_cast(u32x2, v);
}
DI u32x4 trread2(LAS char* p, int hi) {
  u32x2 a = trread(p), b = trread(p + hi);
  return u32x4{a[0], a[1], b[0], b[1]};
}
DI f32x4 mfma16(u32x4 a, u32x4 b, f32x4 c) {
  return __builtin_amdgcn_mfma_f32_16x16x32_bf16(__builtin_bit_cast(bf16x8, a), __builtin_bit_cast(bf16x8, b), c, 0, 0, 0);
}
DI void mfma16a(f32x4& c, const u32x4& a, const u32x4& b) {
  asm volatile("v_mfma_f32_16x16x32_bf16 %0, %1, %2, %0" : "+v"(c) : "v"(a), "v"(b));
}
DI void dma16(const void* g, int ldsaddr) {
  asm volatile("s_mov_b32 m0, %1\n\ts_nop 0\n\tglobal_load_lds_dwordx4 %0, off" ::"v"(g), "s"(ldsaddr) : "memory");
}
DI void dma4(const void* g, int ldsaddr) {
  asm volatile("s_mov_b32 m0, %1\n\ts_nop 0\n\tglobal_load_lds_dword %0, off" ::"v"(g), "s"(ldsaddr) : "memory");
}
template <int OFF = 0> DI void dma16s(unsigned voff, const char* sbase, int ldsaddr) {
  asm volatile("s_mov_b32 m0, %2\n\ts_nop 0\n\tglobal_load_lds_dwordx4 %0, %1 offset:%3" ::"v"(voff), "s"(sbase), "s"(ldsaddr - OFF), "i"(OFF) : "memory");
}
DI void dma16s_q(unsigned voff, const char* sbase, int ldsaddr) {
  asm volatile("s_mov_b32 m0, %2\n\ts_mov_b64 exec, 0xffff\n\tglobal_load_lds_dwordx4 %0, %1\n\ts_mov_b64 exec, -1" ::"v"(voff), "s"(sbase),
               "s"(ldsaddr)
               : "memory");
}
template <int C, int OFF = 0> DI void dma16s_if(int sel, unsigned voff, const char* sbase, int ldsaddr) {
  asm volatile("s_cmp_lg_u32 %3, %4\n\ts_cbranch_scc1 .Lskip%=\n\ts_mov_b32 m0, %2\n\ts_nop 0\n\tglobal_load_lds_dwordx4 %0, %1 offset:%5\n.Lskip%=:" ::"v"(voff),
               "s"(sbase), "s"(ldsaddr - OFF), "s"(sel), "i"(C), "i"(OFF)
               : "memory", "scc");
}
template <int C> DI void dma16s_q_if(int sel, unsigned voff, const char* sbase, int ldsaddr) {
  asm volatile("s_cmp_lg_u32 %3, %4\n\ts_cbranch_scc1 .Lskip%=\n\ts_mov_b32 m0, %2\n\ts_mov_b64 exec, 0xffff\n\tglobal_load_lds_dwordx4 %0, %1\n\ts_mov_b64 exec, -1\n.Lskip%=:" ::"v"(voff),
               "s"(sbase), "s"(ldsaddr), "s"(sel), "i"(C)
               : "memory", "scc");
}
DI int opaque(int v) { asm volatile("" : "+v"(v)); return v; }
DI void keep3(unsigned (&v)[3]) { asm volatile("" ::"v"(v[0]), "v"(v[1]), "v"(v[2])); }
DI void pin4(f32x4& v) { asm volatile("" : "+v"(v)); }
DI void pinu(u32x4& v) { asm volatile("" : "+v"(v)); }
DI void gst(char* p, u32x4 v) {
  asm volatile("global_store_dwordx4 %0, %1, off sc1\n\ts_nop 1" ::"v"(p), "v"(v) : "memory");
}
DI void gst_wt(char* p, u32x4 v) { asm volatile("global_store_dwordx4 %0, %1, off sc1\n\ts_nop 1" ::"v"(p), "v"(v) : "memory"); }
DI void gst(char* p, u32x2 v) {
  asm volatile("global_store_dwordx2 %0, %1, off sc1\n\ts_nop 1" ::"v"(p), "v"(v) : "memory");
}
DI void pinf(float& v) { asm volatile("" : "+v"(v)); }
#define WAIT_V(n) asm volatile("s_waitcnt vmcnt(%0)" ::"i"(n) : "memory")
#define STAMP_DECL
#define STAMP(k, i)
#define STAMP_FLUSH(k)
template <int NL> DI unsigned code_prefetch(int idx, int tid) {
  unsigned long long pc;
  asm volatile("s_getpc_b64 %0" : "=s"(pc));
  unsigned v = 0;
  if (tid < NL) v = *(const GAS unsigned*)(pc + (unsigned long long)(idx * NL + tid) * 64);
  return v;
}
DI const char* uptr(const char* p) {
  const unsigned long long v = (unsigned long long)p;
  const unsigned lo = __builtin_amdgcn_readfirstlane((unsigned)v), hi = __builtin_amdgcn_readfirstlane((unsigned)(v >> 32));
  return (const char*)(((unsigned long long)hi << 32) | lo);
}
#define BAR() __builtin_amdgcn_s_barrier()
#define SCHED() __builtin_amdgcn_sched_barrier(0)
#define K1_PF 15
#define K2_PF 3

#define S_WARM 40
#define S_PT 4
constexpr int NB = 1;
constexpr int S_SIDE = 16;
constexpr int xtra(int s) { return (s < 2 ? NB : 0) + (s == S_SIDE ? 3 : 0); }
#define PD 3
constexpr int g_first(int st) { return st < PD - 3 ? 3 + 2 * st : st + PD; }
constexpr int g_count(int st) { return st < PD - 3 ? 2 : 1; }
constexpr int pend_at(int s) {
  int n = 0;
  bool seen = false;
  for (int j = 0; j < 3; ++j) { if (seen) n += 4; if (j == s + 1) seen = true; if (j >= 1 && seen) n += NB; }
  for (int st = 0; st < s; ++st) {
    for (int j = g_first(st); j < g_first(st) + g_count(st); ++j)
      if (j < 20) { if (seen) n += 4; if (j == s + 1) seen = true; }
    if (seen) n += xtra(st);
  }
  return n;
}
DI const char* warm_addr(int n, int j, const void* px, const void* pq, const void* pg, const void* pu) {
  n = min(n, 21119);
  const int xr = n / 3, xl = n - 3 * xr;
  const int m = max(n - 6144, 0), r = m / 78, c = m - 78 * r;
  const unsigned k = 64 + 192 * j + r;
  unsigned long long a = (unsigned long long)pq + k * 3840u + c * 128u;
  a = c >= 30 ? (unsigned long long)pg + k * 3072u + (c - 30) * 128u : a;
  a = c >= 54 ? (unsigned long long)pu + k * 3072u + (c - 54) * 128u : a;
  a = n < 6144 ? (unsigned long long)px + xr * 1280u + (1 + 3 * j + xl) * 128u : a;
  return (const char*)a;
}
DI const char* warm_addr2(int n, const void* pd, const void* po) {
  n = min(n, 21759);
  return (const char*)((n < 15360 ? (unsigned long long)pd : (unsigned long long)po - 15360ull * 128) + (unsigned long long)n * 128);
}

__global__ __launch_bounds__(512, 2) void k1_proj(const __hip_bfloat16* __restrict__ x_, const __hip_bfloat16* __restrict__ wqkv_,
                                                  const __hip_bfloat16* __restrict__ wphi_, const __hip_bfloat16* __restrict__ wo_,
                                                  const __hip_bfloat16* __restrict__ wg_, const __hip_bfloat16* __restrict__ wu_,
                                                  const __hip_bfloat16* __restrict__ wd_, char* __restrict__ ws) {
  extern __shared__ __attribute__((aligned(16))) char smem[];
  LAS char* L = (LAS char*)smem;
  const int lb = (int)(size_t)L;
  STAMP_DECL;
  STAMP(0, 0);
  int tid = threadIdx.x, l = tid & 63;
  const int w = __builtin_amdgcn_readfirstlane(tid >> 6);
  const int tq = w & 3, fgi = w >> 2;
  const int b = blockIdx.x, xcd = b & 7, idx = b >> 3;
  const unsigned cpf = code_prefetch<K1_PF>(idx, tid);
  const int tg = (xcd & 1) * 4 + (idx & 3);
  const int cg = (xcd >> 1) * 8 + (idx >> 2);
  const int ga = cg >> 2, bt = cg & 3;
  const int kind = bt == 0 ? 1 + fgi : ((bt == 1 && fgi == 0) ? 0 : 3);
  const int mj = bt == 1 ? ga : 8 + 4 * ga + 2 * (bt - 2) + fgi;
  const int mbase = min(40 * mj, 1496);

  unsigned xo[2];
#pragma unroll
  for (int i = 0; i < 2; ++i) {
    const int c = 128 * w + 64 * i + l, tok = c >> 2, slot = c & 3;
    const int swz = (0x78 >> (2 * ((tok >> 2) & 3))) & 3;
    xo[i] = tok * 1280 + 16 * (slot ^ swz);
  }
  const char* xs = uptr((const char*)x_ + (size_t)tg * 256 * 1280);
  const int wsub = tq >> 1;
  const int rs = kind == 3 ? 3072 : 3840;
  unsigned wo[2];
#pragma unroll
  for (int i = 0; i < 2; ++i) {
    const int c = min(64 * i + l, 79), pl = c / 5, ch = c - pl * 5;
    const int pos = 16 * (tq & 1) + pl;
    const int krow = (pos & 19) | ((pos & 4) << 1) | ((pos & 8) >> 1);
    wo[i] = krow * rs + 16 * ch;
  }
  const char* wsrc = uptr(kind == 3 ? (const char*)(wsub ? wu_ : wg_) + mbase * 2
                                    : (const char*)wqkv_ + (640 * kind + 80 * ga + 40 * wsub) * 2);
  const int winc = 32 * rs;
  const int xdst = lb + L_RING + w * 2048;
  const int wdst = lb + L_RING + XB + fgi * WREG + wsub * SUB + (tq & 1) * 1280;

  auto issue_op = [&](auto S, auto O) {
    constexpr int s = decltype(S)::value, o = decltype(O)::value;
    if constexpr (s < 20) {
      if constexpr (o < 2) dma16s<s * 64>(xo[o], xs, xdst + (s % NS) * STG + o * 1024);
      if constexpr (o == 2) dma16s(wo[0], wsrc, wdst + (s % NS) * STG);
      if constexpr (o == 3) { dma16s_q(wo[1], wsrc, wdst + (s % NS) * STG + 1024); wsrc += winc; }
    }
  };
  auto issue_stage = [&](auto S) { sfor<0, 4>([&](auto O) { issue_op(S, O); }); };
  issue_stage(IC<0>{});
  BAR();
  issue_stage(IC<1>{});
  const int gw = b * 8 + w;
  dma4(warm_addr(gw * 11 + min(l, 10), 0, x_, wqkv_, wg_, wu_), lb + L_DUMMY);
  const int fr1 = gw + 2048 < 2880 ? gw + 2048 : gw;
  const int wsel = w < 5 ? w : w - 5;
  auto side_dma = [&]() {
#pragma unroll
    for (int i = 0; i < 2; ++i) {
      const int fr = i ? fr1 : gw;
      const int fg = fr / 360, rem = fr - fg * 360, ks = rem / 5, nt = rem - ks * 5;
      const int slot = l >> 1, g_ = slot >> 3, j = slot & 7;
      const int u = (ks - 48) / 3, s_ = (ks - 48) - u * 3;
      const int id = min(8 * s_ + j, 19), e = id / 5, v = id - e * 5;
      const int d = v < 4 ? 4 * g_ + v : 16 + g_;
      const int row = ks < 48 ? 32 * ks + slot : (4 * u + e) * 20 + d;
      const char* pb_ = uptr(ks < 48 ? (const char*)wd_ : (const char*)wo_);
      dma16s(row * 1280 + (80 * fg + 16 * nt + 8 * (l & 1)) * 2, pb_, lb + L_PT + (w * 2 + i) * 1024);
    }
    dma16s(ga * 3200 + min((w & 3) * 64 + l, 199) * 16, (const char*)wphi_, lb + L_WPHI + (w & 3) * 1024);
  };

  int tok = l & 15, g = l >> 4, q = (l & 15) >> 2, p = l & 3;
  auto relane = [&]() { tid = opaque(threadIdx.x); l = tid & 63; tok = l & 15; g = l >> 4; q = (l & 15) >> 2; p = l & 3; };
  const int abase = L_RING + XB + fgi * WREG + (p >> 1) * SUB + (16 * (g >> 1) + 4 * (g & 1) + q) * 80 + 40 * (p & 1);
  int ab[2], cb[2], bb[2][2];
  {
    const int swz = (0x78 >> (2 * (tok >> 2))) & 3;
#pragma unroll
    for (int h = 0; h < 2; ++h) {
      ab[h] = opaque(abase + h * 2 * STG);
      cb[h] = opaque(abase + 8 * tq + h * 2 * STG);
#pragma unroll
      for (int i = 0; i < 2; ++i) bb[h][i] = opaque(L_RING + (64 * tq + 32 * (i ^ fgi) + tok) * 64 + 16 * (g ^ swz) + h * 2 * STG);
    }
  }

  f32x4 acc[5][4];
#pragma unroll
  for (int t = 0; t < 5; ++t)
#pragma unroll
    for (int i = 0; i < 4; ++i) acc[t][i] = f32x4{0.f, 0.f, 0.f, 0.f};
  f32x4 csa = f32x4{0.f, 0.f, 0.f, 0.f}, csb = f32x4{0.f, 0.f, 0.f, 0.f};
  f32x4 sxa[2] = {f32x4{0.f, 0.f, 0.f, 0.f}, f32x4{0.f, 0.f, 0.f, 0.f}};
  f32x4 gra[2] = {f32x4{0.f, 0.f, 0.f, 0.f}, f32x4{0.f, 0.f, 0.f, 0.f}};
  u32x4 ONES4 = u32x4{0x3F803F80u, 0x3F803F80u, 0x3F803F80u, 0x3F803F80u};
  asm volatile("" : "+v"(ONES4));
#pragma unroll
  for (int t = 0; t < 5; ++t)
#pragma unroll
    for (int i = 0; i < 4; ++i) pin4(acc[t][i]);
  pin4(csa); pin4(csb); pin4(sxa[0]); pin4(sxa[1]); pin4(gra[0]); pin4(gra[1]);
  u32x4 fc[2];
  auto readC = [&](auto S) {
    constexpr int s = decltype(S)::value;
    constexpr int sl = s % NS, h = sl >= 2 ? 1 : 0;
    fc[s & 1] = trread2(L + cb[h] + (sl - 2 * h) * STG, 640);
  };

  u32x4 fa[2][5], fb[2][4];
  auto readA = [&](auto S, auto T) {
    constexpr int s = decltype(S)::value, t = decltype(T)::value;
    constexpr int sl = s % NS, h = sl >= 2 ? 1 : 0;
    fa[s & 1][t] = trread2(L + ab[h] + (sl - 2 * h) * STG + 8 * t, 640);
  };
  auto readB = [&](auto S, auto I) {
    constexpr int s = decltype(S)::value, i = decltype(I)::value;
    constexpr int sl = s % NS, h = sl >= 2 ? 1 : 0;
    fb[s & 1][i] = *(LAS u32x4*)(L + bb[h][i >> 1] + (sl - 2 * h) * STG + (i & 1) * 1024);
  };

  issue_stage(IC<2>{});
  dma4(warm_addr(gw * 11 + min(l, 10), 1, x_, wqkv_, wg_, wu_), lb + L_DUMMY);
  STAMP(0, 1);
  WAIT_V(8 + 2 * NB);
  BAR();
  STAMP(0, 2);
  sfor<0, 4>([&](auto I) { readB(IC<0>{}, I); });
  sfor<0, 5>([&](auto T) { readA(IC<0>{}, T); });
  readC(IC<0>{});
  if (tid < 64) *(LAS u32x4*)(L + L_ZERO + tid * 16) = u32x4{0, 0, 0, 0};
  SCHED();

  sfor<0, 20>([&](auto S) {
    constexpr int s = decltype(S)::value;
    if constexpr (s + 1 < 20) {
      constexpr int pend = pend_at(s);
      asm volatile("s_waitcnt vmcnt(%0) lgkmcnt(0)" ::"i"(pend) : "memory");
      BAR();
    }
    sfor<0, 5>([&](auto T) {
      constexpr int t = decltype(T)::value;
      sfor<0, 4>([&](auto I) {
        constexpr int i = decltype(I)::value;
        mfma16a(acc[t][i], fa[s & 1][t], fb[s & 1][i]);
        if constexpr (s + 1 < 20) {
          if constexpr (t == 0) readB(IC<s + 1>{}, I);
          if constexpr (i == 1) readA(IC<s + 1>{}, T);
        }
        if constexpr (g_count(s) == 2) {
          if constexpr ((i & 1) && t < 4) issue_op(IC<g_first(s) + (t >> 1)>{}, IC<2 * (t & 1) + (i >> 1)>{});
        } else {
          if constexpr (i == 3 && t < 4) issue_op(IC<g_first(s)>{}, T);
        }
        if constexpr (t == 4 && i == 0 && s + 1 < 20) readC(IC<s + 1>{});
        if constexpr (t == 2 && i == 3) mfma16a(csa, fc[s & 1], ONES4);
        if constexpr (t == 1 && i == 3) mfma16a(sxa[0], ONES4, fb[s & 1][0]);
        if constexpr (t == 3 && i == 3) mfma16a(sxa[1], ONES4, fb[s & 1][1]);
        if constexpr (t == 0 && i == 3) mfma16a(gra[0], fb[s & 1][0], fb[s & 1][0]);
        if constexpr (t == 4 && i == 3) mfma16a(gra[1], fb[s & 1][1], fb[s & 1][1]);
        SCHED();
      });
    });
    if ((s & 3) == tq) mfma16a(csb, fa[s & 1][4], ONES4);
    if constexpr (s == 0) { relane(); dma4(warm_addr(gw * 11 + min(l, 10), 2, x_, wqkv_, wg_, wu_), lb + L_DUMMY); }
    if constexpr (s == 1) { relane(); dma4(warm_addr2(gw * 11 + min(l, 10), wd_, wo_), lb + L_DUMMY); }
    if constexpr (s == S_SIDE) { relane(); side_dma(); }
#define ST_A 4
#define ST_B 9
#define ST_C 14
    if constexpr (s == ST_A) STAMP(0, 3);
    if constexpr (s == ST_B) STAMP(0, 4);
    if constexpr (s == ST_C) STAMP(0, 5);
    SCHED();
  });
  asm volatile("s_nop 7\n\ts_nop 7" ::: "memory");
  SCHED();
  STAMP(0, 6);
  relane();
  if (b < 210 && tid < 64) *(unsigned*)(ws + OFF_KVT + b * 256 + tid * 4) = 0xFFFFFFFFu;

  WAIT_V(0);
#pragma unroll
  for (int i = 0; i < 2; ++i) {
    const u32x4 v = trread2(L + L_PT + (w * 2 + i) * 1024 + (8 * g + q) * 32 + 8 * p, 128);
    gst_wt(ws + OFF_WT + (size_t)(i ? fr1 : gw) * 1024 + l * 16, v);
  }
#pragma unroll
  for (int i = 0; i < 2; ++i) {
    const float c = p == 0 ? gra[i][0] : (p == 1 ? gra[i][1] : (p == 2 ? gra[i][2] : gra[i][3]));
    const float a = sxa[i][0];
    const float mean = a * (1.f / 640.f);
    const float var = fmaxf(c * (1.f / 640.f) - mean * mean, 0.f);
    if (g == q) *(LAS f32x2*)(L + L_ST + (64 * tq + 16 * ((i + 2 * fgi) & 3) + tok) * 8) = f32x2{-mean, var + 1e-5f};
  }
  if (tok == 0) {
    *(LAS f32x4*)(L + L_CSS + (fgi * 128 + tq * 16 + 4 * g) * 4) = csa;
    *(LAS f32x4*)(L + L_CSS + (fgi * 128 + 64 + tq * 16 + 4 * g) * 4) = csb;
  }
  __syncthreads();
  float nmean[4], vare[4], rstd[4];
#pragma unroll
  for (int i = 0; i < 4; ++i) {
    const f32x2 v = *(LAS f32x2*)(L + L_ST + (64 * tq + 16 * ((i + 2 * fgi) & 3) + tok) * 8);
    nmean[i] = v[0]; vare[i] = v[1];
    rstd[i] = __builtin_amdgcn_rsqf(v[1]);
  }
#pragma unroll
  for (int t = 0; t < 5; ++t) {
    f32x4 c4 = *(LAS f32x4*)(L + L_CSS + (fgi * 128 + t * 16 + 4 * g) * 4);
    if (t == 4) {
#pragma unroll
      for (int k = 1; k < 4; ++k) c4 += *(LAS f32x4*)(L + L_CSS + (fgi * 128 + 64 + k * 16 + 4 * g) * 4);
    }
#pragma unroll
    for (int i = 0; i < 4; ++i)
#pragma unroll
      for (int c = 0; c < 4; ++c) acc[t][i][c] = fmaf(nmean[i], c4[c], acc[t][i][c]);
  }
  STAMP(0, 7);
  const int tile0 = tg * 16 + tq * 4;

  if (kind == 3) {
    const int hi = l >> 5, pp = g & 1;
#pragma unroll
    for (int i = 0; i < 2; ++i) {
      const float rs_ = hi ? rstd[i + 2] : rstd[i], ve = hi ? vare[i + 2] : vare[i];
      const float c1 = -2.0f * 0.7978845608028654f * 1.4426950408889634f;
      const float cA = c1 * rs_, cB = c1 * 0.044715f * rs_ * rs_ * rs_;
      unsigned hp[10];
#pragma unroll
      for (int t = 0; t < 5; ++t) {
#pragma unroll
        for (int c2 = 0; c2 < 2; ++c2) {
          f32x2 gg, uu;
#pragma unroll
          for (int c = 0; c < 2; ++c) {
            const float va = acc[t][i][2 * c2 + c], vb = acc[t][i + 2][2 * c2 + c];
            const auto sw = __builtin_amdgcn_permlane32_swap(__float_as_uint(va), __float_as_uint(vb), false, false);
            const unsigned s0 = sw[0], s1 = sw[1];
            gg[c] = __uint_as_float(s0); uu[c] = __uint_as_float(s1);
          }
          const f32x2 arg = __builtin_elementwise_fma(gg * gg, f32x2{cB, cB}, f32x2{cA, cA}) * gg;
          const f32x2 e = f32x2{__builtin_amdgcn_exp2f(arg[0]), __builtin_amdgcn_exp2f(arg[1])};
          const f32x2 dn = __builtin_elementwise_fma(e, f32x2{ve, ve}, f32x2{ve, ve});
          const f32x2 rr = f32x2{__builtin_amdgcn_rcpf(dn[0]), __builtin_amdgcn_rcpf(dn[1])};
          const f32x2 hv = gg * uu * rr;
          hp[2 * t + c2] = pk2(hv[0], hv[1]);
        }
      }
      const int tl = tile0 + ((i + 2 * hi + 2 * fgi) & 3);
      char* dst = ws + OFF_H + (size_t)tl * 49152 + (mbase >> 3) * 256 + tok * 16;
      if (pp == 0) {
        gst(dst, u32x4{hp[0], hp[1], hp[2], hp[3]});
        gst(dst + 256, u32x4{hp[4], hp[5], hp[6], hp[7]});
        gst(dst + 512, u32x2{hp[8], hp[9]});
      } else {
        gst(dst + 520, u32x2{hp[0], hp[1]});
        gst(dst + 768, u32x4{hp[2], hp[3], hp[4], hp[5]});
        gst(dst + 1024, u32x4{hp[6], hp[7], hp[8], hp[9]});
      }
    }
  } else if (kind == 2) {
#pragma unroll
    for (int i = 0; i < 4; ++i) {
      const int trow = 64 * tq + 16 * ((i + 2) & 3) + tok;
#pragma unroll
      for (int t = 0; t < 5; ++t)
        *(LAS u32x2*)(L + L_V + trow * V_PITCH + 40 * g + 8 * t) =
            u32x2{pk2(acc[t][i][0] * rstd[i], acc[t][i][1] * rstd[i]), pk2(acc[t][i][2] * rstd[i], acc[t][i][3] * rstd[i])};
      if (g == 0) *(LAS u32x2*)(L + L_V + trow * V_PITCH + 160) = u32x2{0x00003F80u, 0u};
    }
  } else {
    u32x4 bq[4][3];
#pragma unroll
    for (int i = 0; i < 4; ++i) {
#pragma unroll
      for (int s = 0; s < 2; ++s)
        bq[i][s] = u32x4{pk2(acc[2 * s][i][0], acc[2 * s][i][1]), pk2(acc[2 * s][i][2], acc[2 * s][i][3]),
                         pk2(acc[2 * s + 1][i][0], acc[2 * s + 1][i][1]), pk2(acc[2 * s + 1][i][2], acc[2 * s + 1][i][3])};
      bq[i][2] = u32x4{pk2(acc[4][i][0], acc[4][i][1]), pk2(acc[4][i][2], acc[4][i][3]), 0u, 0u};
    }
    const int wb = (p == g) ? (L_WPHI + g * 800 + q * 40) : L_ZERO;
    f32x4 pq[5][4];
#pragma unroll
    for (int tt = 0; tt < 5; ++tt) {
      const u32x4 a0 = trread2(L + wb + 8 * tt, 160), a1 = trread2(L + wb + 8 * tt + 320, 160);
      const u32x2 a2l = trread(L + wb + 8 * tt + 640);
      const u32x4 a2 = u32x4{a2l[0], a2l[1], 0u, 0u};
#pragma unroll
      for (int i = 0; i < 4; ++i) {
        f32x4 o = f32x4{0.f, 0.f, 0.f, 0.f};
        o = mfma16(a0, bq[i][0], o);
        o = mfma16(a1, bq[i][1], o);
        o = mfma16(a2, bq[i][2], o);
        pq[tt][i] = o;
      }
    }
#pragma unroll
    for (int i = 0; i < 4; ++i) {
      const float c1 = rstd[i] * 1.4426950408889634f;
      float mx = pq[0][i][0], mn = pq[0][i][0];
#pragma unroll
      for (int f = 1; f < 20; ++f) { mx = fmaxf(mx, pq[f >> 2][i][f & 3]); mn = fminf(mn, pq[f >> 2][i][f & 3]); }
      const float bx = -mx * c1, bn = mn * c1;
      f32x2 ep[10], en[10], sp2 = f32x2{0.f, 0.f}, sn2 = f32x2{0.f, 0.f};
#pragma unroll
      for (int f = 0; f < 10; ++f) {
        const f32x2 pv = f32x2{pq[f >> 1][i][2 * (f & 1)], pq[f >> 1][i][2 * (f & 1) + 1]};
        const f32x2 ap = __builtin_elementwise_fma(pv, f32x2{c1, c1}, f32x2{bx, bx});
        const f32x2 an = __builtin_elementwise_fma(pv, f32x2{-c1, -c1}, f32x2{bn, bn});
        ep[f] = f32x2{__builtin_amdgcn_exp2f(ap[0]), __builtin_amdgcn_exp2f(ap[1])};
        en[f] = f32x2{__builtin_amdgcn_exp2f(an[0]), __builtin_amdgcn_exp2f(an[1])};
        sp2 += ep[f]; sn2 += en[f];
      }
      const float rp = __builtin_amdgcn_rcpf(sp2[0] + sp2[1]), rn = __builtin_amdgcn_rcpf(sn2[0] + sn2[1]);
      unsigned o[20];
#pragma unroll
      for (int f = 0; f < 10; ++f) {
        const f32x2 a = ep[f] * f32x2{rp, rp}, c = en[f] * f32x2{rn, rn};
        o[f] = pk2(a[0], a[1]);
        o[10 + f] = pk2(c[0], c[1]);
      }
      if (kind == 0) {
        char* dst = ws + OFF_PQ + (size_t)((tile0 + i) * 8 + ga) * 5120 + tok * 16;
#pragma unroll
        for (int ci = 0; ci < 4; ++ci) gst(dst + g * 1024 + ci * 256, u32x4{o[4 * ci], o[4 * ci + 1], o[4 * ci + 2], o[4 * ci + 3]});
        gst(dst + 4096 + g * 256, u32x4{o[16], o[17], o[18], o[19]});
      } else {
        const int trow = 64 * tq + 16 * i + tok;
#pragma unroll
        for (int ci = 0; ci < 5; ++ci)
          *(LAS u32x4*)(L + L_PK + trow * PK_PITCH + g * 80 + ci * 16) = u32x4{o[4 * ci], o[4 * ci + 1], o[4 * ci + 2], o[4 * ci + 3]};
      }
    }
  }
  if (bt == 0) {
    __syncthreads();
    const int e = w >> 1, bh = w & 1;
    const int rowoff = 8 * g + q;
    LAS char* pa = L + L_PK + rowoff * PK_PITCH + e * 80 + 8 * p;
    LAS char* pa2 = L + L_PK + rowoff * PK_PITCH + e * 80 + 64 + 8 * (p & 1);
    LAS char* pb = L + L_V + rowoff * V_PITCH + (bh == 0 ? e * 40 + 8 * p : (p == 0 ? e * 40 + 32 : 160));
    f32x4 d[3];
#pragma unroll
    for (int k = 0; k < 3; ++k) d[k] = f32x4{0.f, 0.f, 0.f, 0.f};
#pragma unroll
    for (int ks = 0; ks < 8; ++ks) {
      const u32x4 bb = trread2(pb + ks * 32 * V_PITCH, 4 * V_PITCH);
      d[0] = mfma16(trread2(pa + ks * 32 * PK_PITCH, 4 * PK_PITCH), bb, d[0]);
      d[1] = mfma16(trread2(pa + ks * 32 * PK_PITCH + 32, 4 * PK_PITCH), bb, d[1]);
      d[2] = mfma16(trread2(pa2 + ks * 32 * PK_PITCH, 4 * PK_PITCH), bb, d[2]);
    }
    const int dcol = 16 * bh + tok;
    if (dcol <= 20) {
      float* dst = (float*)(ws + OFF_KVP) + ((size_t)(tg * 32 + 4 * ga + e) * 21 + dcol) * 40 + 4 * g;
      *(f32x4*)(dst) = d[0];
      *(f32x4*)(dst + 16) = d[1];
      if (g < 2) *(f32x4*)(dst + 32) = d[2];
    }
  }
  asm volatile("" ::"v"(cpf));
  STAMP(0, 8);
  WAIT_V(0);
  STAMP_FLUSH(0);
}

constexpr int K2_LDS = 163840;
__global__ __launch_bounds__(512, 2) void k2_out(const __hip_bfloat16* __restrict__ x_, char* __restrict__ ws,
                                                 __hip_bfloat16* __restrict__ out_) {
  extern __shared__ __attribute__((aligned(16))) char smem[];
  LAS char* L = (LAS char*)smem;
  const int tid = threadIdx.x, l = tid & 63;
  const int w = __builtin_amdgcn_readfirstlane(tid >> 6);
  const int b = blockIdx.x, xcd = b & 7, idx = b >> 3;
  STAMP_DECL;
  STAMP(1, 0);
  const __amdgpu_buffer_rsrc_t rs = __builtin_amdgcn_make_buffer_rsrc((void*)ws, 0, (int)WS_BYTES, 0x00020000);
  const bool red = (w == 0 && b < 210);
  f32x2 pv[NP];
  if (red) {
    const GAS f32x2* pp = (const GAS f32x2*)(ws + OFF_KVP) + b * 64 + l;
#pragma unroll
    for (int k = 0; k < NP; ++k) pv[k] = pp[k * 13440];
  }
  SCHED();
  BAR();
  const unsigned cpf = code_prefetch<K2_PF>(idx, tid);
  const int tg = xcd * 4 + (idx & 3);
  const int fg = idx >> 2;
  const int vo = l * 16;
  const int so_h = (int)OFF_H + tg * 4 * 49152 + 6 * w * 1024;
  const int so_a = (int)OFF_WT + fg * 72 * 5120 + 6 * w * 5120;
  const int so_p = (int)OFF_PQ + (tg * 32 + w) * 5120;
  const int so_o = (int)OFF_WT + fg * 72 * 5120 + (48 + 3 * w) * 5120;
  auto ldg = [&](int so) -> u32x4 { return __builtin_amdgcn_raw_buffer_load_b128(rs, vo, so, 0); };
  f32x4 acc[5][4];
#pragma unroll
  for (int nt = 0; nt < 5; ++nt)
#pragma unroll
    for (int i = 0; i < 4; ++i) { acc[nt][i] = f32x4{0.f, 0.f, 0.f, 0.f}; pin4(acc[nt][i]); }

  u32x4 fa[6][5], fb[6][4], pf[4][5], ca[3][5];
  auto ld = [&](auto K) {
    constexpr int k = decltype(K)::value;
#pragma unroll
    for (int i = 0; i < 4; ++i) fb[k][i] = ldg(so_h + i * 49152 + k * 1024);
#pragma unroll
    for (int nt = 0; nt < 5; ++nt) fa[k][nt] = ldg(so_a + k * 5120 + nt * 1024);
    SCHED();
  };
  auto mm = [&](auto K) {
    constexpr int k = decltype(K)::value;
#pragma unroll
    for (int nt = 0; nt < 5; ++nt)
#pragma unroll
      for (int i = 0; i < 4; ++i) {
        mfma16a(acc[nt][i], fa[k][nt], fb[k][i]);
      }
    SCHED();
  };
  auto ldp = [&](int hp) {
#pragma unroll
    for (int i = 0; i < 4; ++i) {
      pf[i][2 * hp] = ldg(so_p + i * 40960 + 2 * hp * 1024);
      pf[i][2 * hp + 1] = ldg(so_p + i * 40960 + (2 * hp + 1) * 1024);
      if (hp == 0) pf[i][4] = ldg(so_p + i * 40960 + 4096);
    }
    SCHED();
  };
  auto ldo = [&](int s) {
#pragma unroll
    for (int nt = 0; nt < 5; ++nt) ca[s][nt] = ldg(so_o + s * 5120 + nt * 1024);
    SCHED();
  };
  const int r = l & 15, g = l >> 4;
  const int row1 = (r & 3) == 0 ? 16 + (r >> 2) : 20;
  const int so_k = (int)OFF_KVT + 4 * w * 1680;
  const int ko0 = r * 80 + g * 16, ko1 = row1 * 80 + g * 16, kt0 = r * 80 + g * 1680 + 64, kt1 = row1 * 80 + g * 1680 + 64;
  u32x4 ka[4][2], kt[2];
  auto ldk = [&](int hp) {
#pragma unroll
    for (int hd = 2 * hp; hd < 2 * hp + 2; ++hd) {
      ka[hd][0] = __builtin_amdgcn_raw_buffer_load_b128(rs, ko0, so_k + hd * 1680, 16);
      ka[hd][1] = __builtin_amdgcn_raw_buffer_load_b128(rs, ko1, so_k + hd * 1680, 16);
    }
    if (hp == 0) {
      kt[0] = __builtin_amdgcn_raw_buffer_load_b128(rs, kt0, so_k, 16);
      kt[1] = __builtin_amdgcn_raw_buffer_load_b128(rs, kt1, so_k, 16);
    }
  };
  auto kbad = [&](int hp) -> bool {
    unsigned mxw = 0u;
#pragma unroll
    for (int k = 0; k < 2; ++k)
#pragma unroll
      for (int j = 0; j < 4; ++j) {
        if (hp == 0) mxw = max(mxw, kt[k][j]);
#pragma unroll
        for (int hd = 2 * hp; hd < 2 * hp + 2; ++hd) mxw = max(mxw, ka[hd][k][j]);
      }
    return __any(mxw == 0xFFFFFFFFu);
  };

  if (red) {
    f32x2 s = pv[0];
#pragma unroll
    for (int k = 1; k < NP; ++k) s += pv[k];
    __builtin_amdgcn_raw_buffer_store_b32(pk2(s[0], s[1]), rs, (int)OFF_KVT + b * 256 + l * 4, 0, 16);
  }
  SCHED();
  STAMP(1, 1);
  ld(IC<0>{}); ld(IC<1>{}); ld(IC<2>{});
  mm(IC<0>{}); ld(IC<3>{});
  asm volatile("" ::"v"(cpf));
  mm(IC<1>{}); ldp(0);
  mm(IC<2>{}); ldk(0); SCHED();
  mm(IC<3>{}); ldk(1); ldp(1);
  STAMP(1, 2);
  unsigned op[4][12];
#pragma unroll
  for (int hp = 0; hp < 2; ++hp) {
    if (__builtin_expect(kbad(hp), 0)) {
      unsigned spin = 0;
      do {
        __builtin_amdgcn_s_sleep(4);
        ldk(hp);
      } while (kbad(hp) && ++spin < (1u << 14));
    }
    if (hp == 0) STAMP(1, 3);
    u32x4 km[2][2];
#pragma unroll
    for (int z = 0; z < 2; ++z)
#pragma unroll
      for (int k = 0; k < 2; ++k) km[z][k] = (g == 2 * hp + z) ? kt[k] : u32x4{0u, 0u, 0u, 0u};
#pragma unroll
    for (int i = 0; i < 4; ++i) {
      float ov[10];
#pragma unroll
      for (int z = 0; z < 2; ++z) {
        const int hd = 2 * hp + z;
        f32x4 o0 = f32x4{0.f, 0.f, 0.f, 0.f}, o1 = f32x4{0.f, 0.f, 0.f, 0.f};
        o0 = mfma16(ka[hd][0], pf[i][hd], o0);
        o1 = mfma16(ka[hd][1], pf[i][hd], o1);
        o0 = mfma16(km[z][0], pf[i][4], o0);
        o1 = mfma16(km[z][1], pf[i][4], o1);
        const float inv = __builtin_amdgcn_rcpf(o1[1] + 1e-6f);
#pragma unroll
        for (int v = 0; v < 4; ++v) ov[5 * z + v] = o0[v] * inv;
        ov[5 * z + 4] = o1[0] * inv;
      }
#pragma unroll
      for (int j = 0; j < 5; ++j) op[i][5 * hp + j] = (unsigned)opaque((int)pk2(ov[2 * j], ov[2 * j + 1]));
      op[i][10] = 0u; op[i][11] = 0u;
      if (i & 1) SCHED();
    }
    SCHED();
    if (hp == 0) ld(IC<4>{});
    else { ld(IC<5>{}); ldo(0); }
  }
  u32x4 ob[3][4];
#pragma unroll
  for (int s = 0; s < 3; ++s)
#pragma unroll
    for (int i = 0; i < 4; ++i) { ob[s][i] = u32x4{op[i][4 * s], op[i][4 * s + 1], op[i][4 * s + 2], op[i][4 * s + 3]}; pinu(ob[s][i]); }
  SCHED();
  mm(IC<4>{}); ldo(1); ldo(2);
  u32x2 xv[3];
#pragma unroll
  for (int k = 0; k < 3; ++k) {
    const int t = min(w + 8 * k, 19), nt = t >> 2, i = t & 3;
    xv[k] = *(const u32x2*)((const char*)x_ + ((size_t)(tg * 64 + i * 16 + (l & 15)) * 640 + fg * 80 + nt * 16 + 4 * (l >> 4)) * 2);
  }
  SCHED();
  mm(IC<5>{});
#pragma unroll
  for (int s = 0; s < 3; ++s) {
#pragma unroll
    for (int nt = 0; nt < 5; ++nt)
#pragma unroll
      for (int i = 0; i < 4; ++i) mfma16a(acc[nt][i], ca[s][nt], ob[s][i]);
  }
  SCHED();
  asm volatile("s_nop 7\n\ts_nop 7" ::: "memory");
  SCHED();
  STAMP(1, 4);
#pragma unroll
  for (int nt = 0; nt < 5; ++nt)
#pragma unroll
    for (int i = 0; i < 4; ++i) *(LAS f32x4*)(L + ((w * 20 + nt * 4 + i) * 64 + l) * 16) = acc[nt][i];
  __syncthreads();
  STAMP(1, 5);
#pragma unroll
  for (int k = 0; k < 3; ++k) {
    const int t = w + 8 * k;
    if (t < 20) {
      f32x4 s = *(LAS f32x4*)(L + (t * 64 + l) * 16);
#pragma unroll
      for (int ww = 1; ww < 8; ++ww) s += *(LAS f32x4*)(L + ((ww * 20 + t) * 64 + l) * 16);
      const int nt = t >> 2, i = t & 3;
      const size_t off = ((size_t)(tg * 64 + i * 16 + (l & 15)) * 640 + fg * 80 + nt * 16 + 4 * (l >> 4)) * 2;
      const float x0 = __builtin_bit_cast(float, xv[k][0] << 16), x1 = __builtin_bit_cast(float, xv[k][0] & 0xffff0000u);
      const float x2 = __builtin_bit_cast(float, xv[k][1] << 16), x3 = __builtin_bit_cast(float, xv[k][1] & 0xffff0000u);
      *(u32x2*)((char*)out_ + off) = u32x2{pk2(s[0] + x0, s[1] + x1), pk2(s[2] + x2, s[3] + x3)};
    }
  }
  STAMP(1, 6);
  STAMP_FLUSH(1);
}

extern "C" void kernel_launch(void* const* d_in, const int* in_sizes, int n_in, void* d_out, int out_size, void* d_ws,
                              size_t ws_size, hipStream_t stream) {
  const __hip_bfloat16* x = (const __hip_bfloat16*)d_in[0];
  const __hip_bfloat16* wqkv = (const __hip_bfloat16*)d_in[1];
  const __hip_bfloat16* wphi = (const __hip_bfloat16*)d_in[2];
  const __hip_bfloat16* wo = (const __hip_bfloat16*)d_in[3];
  const __hip_bfloat16* wg = (const __hip_bfloat16*)d_in[5];
  const __hip_bfloat16* wu = (const __hip_bfloat16*)d_in[6];
  const __hip_bfloat16* wd = (const __hip_bfloat16*)d_in[7];
  (void)hipFuncSetAttribute((const void*)k1_proj, hipFuncAttributeMaxDynamicSharedMemorySize, K1_LDS);
  (void)hipFuncSetAttribute((const void*)k2_out, hipFuncAttributeMaxDynamicSharedMemorySize, K2_LDS);
  k1_proj<<<256, 512, K1_LDS, stream>>>(x, wqkv, wphi, wo, wg, wu, wd, (char*)d_ws);
  k2_out<<<256, 512, K2_LDS, stream>>>(x, (char*)d_ws, (__hip_bfloat16*)d_out);
}
```

```cpp
#define SC1ST
#include <hip/hip_runtime.h>
#include <hip/hip_bf16.h>
#include <stdint.h>

#define GAS __attribute__((address_space(1)))
#define LAS __attribute__((address_space(3)))
#define DI __device__ __forceinline__
typedef __bf16 bf16x8 __attribute__((ext_vector_type(8)));
typedef __bf16 bf16x2 __attribute__((ext_vector_type(2)));
typedef short s16x4 __attribute__((ext_vector_type(4)));
typedef float f32x4 __attribute__((ext_vector_type(4)));
typedef float f32x2 __attribute__((ext_vector_type(2)));
typedef unsigned u32x4 __attribute__((ext_vector_type(4)));
typedef unsigned u32x2 __attribute__((ext_vector_type(2)));

extern "C" __device__ unsigned long long llvm_dispatch_id() __asm("llvm.amdgcn.dispatch.id");
template <int N> struct IC { static constexpr int value = N; };
template <int B, int E, class F> DI void sfor(F&& f) {
  if constexpr (B < E) { f(IC<B>{}); sfor<B + 1, E>(f); }
}

constexpr unsigned OFF_H = 0;
constexpr unsigned OFF_PQ = 6291456;
constexpr unsigned OFF_WT = 11534336;
constexpr unsigned OFF_KVP = 14483456;
constexpr unsigned OFF_KVT = 15343616;
constexpr unsigned OFF_ST = 15397376;
constexpr unsigned OFF_CS = OFF_ST + 32768;
constexpr unsigned WS_BYTES = OFF_CS + 327680;
constexpr int NP = 8;

constexpr int SUB = 2688;
constexpr int WREG = SUB + 2560;
constexpr int WSTG = 2 * WREG;
constexpr int XSTG = 32768;
constexpr int NXS = 3, NWS = 4;
constexpr int L_WPHI = 0;
constexpr int L_ZERO = L_WPHI + 4096;
constexpr int L_ST = L_ZERO + 1024;
constexpr int L_CSS = L_ST + 2048;
constexpr int L_DUMMY = L_CSS + 2048;
constexpr int L_RING = L_CSS + 2560;
constexpr int L_XR = L_RING, L_WR = L_XR + NXS * XSTG;
constexpr int L_PT = L_XR + XSTG;
constexpr int K1_LDS = L_WR + NWS * WSTG;
constexpr int PK_PITCH = 336, V_PITCH = 176;
constexpr int L_PK = L_RING, L_V = L_PK + 256 * PK_PITCH;

DI unsigned pk2(float a, float b) {
  bf16x2 v; v[0] = (__bf16)a; v[1] = (__bf16)b;
  return __builtin_bit_cast(unsigned, v);
}
DI float dot2(unsigned a, unsigned b, float c) {
  return __builtin_amdgcn_fdot2_f32_bf16(__builtin_bit_cast(bf16x2, a), __builtin_bit_cast(bf16x2, b), c, false);
}
DI u32x2 trread(LAS char* p) {
  s16x4 v = __builtin_amdgcn_ds_read_tr16_b64_v4i16((LAS s16x4*)p);
  return __builtin_bit_cast(u32x2, v);
}
DI u32x4 trread2(LAS char* p, int hi) {
  u32x2 a = trread(p), b = trread(p + hi);
  return u32x4{a[0], a[1], b[0], b[1]};
}
DI f32x4 mfma16(u32x4 a, u32x4 b, f32x4 c) {
  return __builtin_amdgcn_mfma_f32_16x16x32_bf16(__builtin_bit_cast(bf16x8, a), __builtin_bit_cast(bf16x8, b), c, 0, 0, 0);
}
DI void mfma16a(f32x4& c, const u32x4& a, const u32x4& b) {
  asm volatile("v_mfma_f32_16x16x32_bf16 %0, %1, %2, %0" : "+v"(c) : "v"(a), "v"(b));
}
DI void dma16(const void* g, int ldsaddr) {
  asm volatile("s_mov_b32 m0, %1\n\ts_nop 0\n\tglobal_load_lds_dwordx4 %0, off" ::"v"(g), "s"(ldsaddr) : "memory");
}
DI void gload16a(u32x4& dst, unsigned voff, const char* sbase) {
  asm volatile("global_load_dwordx4 %0, %1, %2" : "=v"(dst) : "v"(voff), "s"(sbase));
}
template <int CTRL, int RM> DI float dpp_add(float v) {
  return v + __int_as_float(__builtin_amdgcn_update_dpp(0, __float_as_int(v), CTRL, RM, 0xf, false));
}
DI float wave_sum(float v) {
  v = dpp_add<0x111, 0xf>(v);
  v = dpp_add<0x112, 0xf>(v);
  v = dpp_add<0x114, 0xf>(v);
  v = dpp_add<0x118, 0xf>(v);
  v = dpp_add<0x142, 0xa>(v);
  v = dpp_add<0x143, 0xc>(v);
  return __int_as_float(__builtin_amdgcn_readlane(__float_as_int(v), 63));
}
DI void dma4(const void* g, int ldsaddr) {
  asm volatile("s_mov_b32 m0, %1\n\ts_nop 0\n\tglobal_load_lds_dword %0, off" ::"v"(g), "s"(ldsaddr) : "memory");
}
template <int OFF = 0> DI void dma16s(unsigned voff, const char* sbase, int ldsaddr) {
  asm volatile("s_mov_b32 m0, %2\n\ts_nop 0\n\tglobal_load_lds_dwordx4 %0, %1 offset:%3" ::"v"(voff), "s"(sbase), "s"(ldsaddr - OFF), "i"(OFF) : "memory");
}
DI void dma16s_q(unsigned voff, const char* sbase, int ldsaddr) {
  asm volatile("s_mov_b32 m0, %2\n\ts_mov_b64 exec, 0xffff\n\tglobal_load_lds_dwordx4 %0, %1\n\ts_mov_b64 exec, -1" ::"v"(voff), "s"(sbase),
               "s"(ldsaddr)
               : "memory");
}
template <int C, int OFF = 0> DI void dma16s_if(int sel, unsigned voff, const char* sbase, int ldsaddr) {
  asm volatile("s_cmp_lg_u32 %3, %4\n\ts_cbranch_scc1 .Lskip%=\n\ts_mov_b32 m0, %2\n\ts_nop 0\n\tglobal_load_lds_dwordx4 %0, %1 offset:%5\n.Lskip%=:" ::"v"(voff),
               "s"(sbase), "s"(ldsaddr - OFF), "s"(sel), "i"(C), "i"(OFF)
               : "memory", "scc");
}
template <int C> DI void dma16s_q_if(int sel, unsigned voff, const char* sbase, int ldsaddr) {
  asm volatile("s_cmp_lg_u32 %3, %4\n\ts_cbranch_scc1 .Lskip%=\n\ts_mov_b32 m0, %2\n\ts_mov_b64 exec, 0xffff\n\tglobal_load_lds_dwordx4 %0, %1\n\ts_mov_b64 exec, -1\n.Lskip%=:" ::"v"(voff),
               "s"(sbase), "s"(ldsaddr), "s"(sel), "i"(C)
               : "memory", "scc");
}
DI int opaque(int v) { asm volatile("" : "+v"(v)); return v; }
DI void keep3(unsigned (&v)[3]) { asm volatile("" ::"v"(v[0]), "v"(v[1]), "v"(v[2])); }
DI void pin4(f32x4& v) { asm volatile("" : "+v"(v)); }
DI void pinu(u32x4& v) { asm volatile("" : "+v"(v)); }
DI void gst(char* p, u32x4 v) {
  asm volatile("global_store_dwordx4 %0, %1, off sc1\n\ts_nop 1" ::"v"(p), "v"(v) : "memory");
}
DI void gst_wt(char* p, u32x4 v) { asm volatile("global_store_dwordx4 %0, %1, off sc1\n\ts_nop 1" ::"v"(p), "v"(v) : "memory"); }
DI void gst(char* p, u32x2 v) {
  asm volatile("global_store_dwordx2 %0, %1, off sc1\n\ts_nop 1" ::"v"(p), "v"(v) : "memory");
}
DI void pinf(float& v) { asm volatile("" : "+v"(v)); }
#define WAIT_V(n) asm volatile("s_waitcnt vmcnt(%0)" ::"i"(n) : "memory")
#define STAMP_DECL
#define STAMP(k, i)
#define STAMP_FLUSH(k)
template <int NL> DI unsigned code_prefetch(int idx, int tid) {
  unsigned long long pc;
  asm volatile("s_getpc_b64 %0" : "=s"(pc));
  unsigned v = 0;
  if (tid < NL) v = *(const GAS unsigned*)(pc + (unsigned long long)(idx * NL + tid) * 64);
  return v;
}
DI const char* uptr(const char* p) {
  const unsigned long long v = (unsigned long long)p;
  const unsigned lo = __builtin_amdgcn_readfirstlane((unsigned)v), hi = __builtin_amdgcn_readfirstlane((unsigned)(v >> 32));
  return (const char*)(((unsigned long long)hi << 32) | lo);
}
#define BAR() __builtin_amdgcn_s_barrier()
#define SCHED() __builtin_amdgcn_sched_barrier(0)
#define K1_PF 15
#define K2_PF 3

#define S_WARM 40
#define S_PT 4
constexpr int NB = 1;
constexpr int S_SIDE = 16;
constexpr int xtra(int s) { return (s < 2 ? NB : 0) + (s == 1 ? 1 : 0) + (s == S_SIDE ? 3 + 4 : 0); }
constexpr int pend_at(int s) {
  int n = 0;
  auto op = [&](int cnt, int need) { if (need <= s + 1) n = 0; else n += cnt; };
  op(4, 0); op(2, 0); op(2, 1); op(NB + 2, 99); op(4, 2); op(2, 2); op(NB, 99);
  for (int st = 0; st < s; ++st) {
    if (st / 2 + 2 < 10) op(2, 2 * (st / 2 + 2));
    if (st + 3 < 20) op(2, st + 3);
    op(xtra(st), 99);
  }
  return n;
}
DI const char* warm_addr(int n, int j, const void* px, const void* pq, const void* pg, const void* pu) {
  n = min(n, 21119);
  const int xr = n / 3, xl = n - 3 * xr;
  const int m = max(n - 6144, 0), r = m / 78, c = m - 78 * r;
  const unsigned k = 64 + 192 * j + r;
  unsigned long long a = (unsigned long long)pq + k * 3840u + c * 128u;
  a = c >= 30 ? (unsigned long long)pg + k * 3072u + (c - 30) * 128u : a;
  a = c >= 54 ? (unsigned long long)pu + k * 3072u + (c - 54) * 128u : a;
  a = n < 6144 ? (unsigned long long)px + xr * 1280u + (1 + 3 * j + xl) * 128u : a;
  return (const char*)a;
}
DI const char* warm_addr2(int n, const void* pd, const void* po) {
  n = min(n, 21759);
  return (const char*)((n < 15360 ? (unsigned long long)pd : (unsigned long long)po - 15360ull * 128) + (unsigned long long)n * 128);
}

__global__ __launch_bounds__(512, 2) void k1_proj(const __hip_bfloat16* __restrict__ x_, const __hip_bfloat16* __restrict__ wqkv_,
                                                  const __hip_bfloat16* __restrict__ wphi_, const __hip_bfloat16* __restrict__ wo_,
                                                  const __hip_bfloat16* __restrict__ wg_, const __hip_bfloat16* __restrict__ wu_,
                                                  const __hip_bfloat16* __restrict__ wd_, char* __restrict__ ws) {
  extern __shared__ __attribute__((aligned(16))) char smem[];
  LAS char* L = (LAS char*)smem;
  const int lb = (int)(size_t)L;
  STAMP_DECL;
  STAMP(0, 0);
  int tid = threadIdx.x, l = tid & 63;
  const int w = __builtin_amdgcn_readfirstlane(tid >> 6);
  const int tq = w & 3, fgi = w >> 2;
  const int b = blockIdx.x, xcd = b & 7, idx = b >> 3;
  const unsigned cpf = code_prefetch<K1_PF>(idx, tid);
  const int tg = (xcd & 1) * 4 + (idx & 3);
  const int cg = (xcd >> 1) * 8 + (idx >> 2);
  const int ga = cg >> 2, bt = cg & 3;
  const int kind = bt == 0 ? 1 + fgi : ((bt == 1 && fgi == 0) ? 0 : 3);
  const int mj = bt == 1 ? ga : 8 + 4 * ga + 2 * (bt - 2) + fgi;
  const int mbase = min(40 * mj, 1496);

  const unsigned xo0 = (32 * w + (l >> 3)) * 1280 + 16 * ((l & 7) ^ ((l >> 3) & 7));
  const char* xs = uptr((const char*)x_ + (size_t)tg * 256 * 1280);
  const int wsub = tq >> 1;
  const int rs = kind == 3 ? 3072 : 3840;
  unsigned wo[2];
#pragma unroll
  for (int i = 0; i < 2; ++i) {
    const int c = min(64 * i + l, 79), pl = c / 5, ch = c - pl * 5;
    const int pos = 16 * (tq & 1) + pl;
    const int krow = (pos & 19) | ((pos & 4) << 1) | ((pos & 8) >> 1);
    wo[i] = krow * rs + 16 * ch;
  }
  const char* wsrc = uptr(kind == 3 ? (const char*)(wsub ? wu_ : wg_) + mbase * 2
                                    : (const char*)wqkv_ + (640 * kind + 80 * ga + 40 * wsub) * 2);
  const int winc = 32 * rs;
  const int xdst = lb + L_XR + w * 4096;
  const int wdst = lb + L_WR + fgi * WREG + wsub * SUB + (tq & 1) * 1280;
  auto issue_x = [&](auto J, auto O) {
    constexpr int j = decltype(J)::value, o = decltype(O)::value;
    if constexpr (j < 10) dma16s<j * 128>(xo0, xs + o * 10240, xdst + (j % NXS) * XSTG + o * 1024);
  };
  auto issue_w = [&](auto S, auto O) {
    constexpr int s = decltype(S)::value, o = decltype(O)::value;
    if constexpr (s < 20) {
      if constexpr (o == 0) dma16s(wo[0], wsrc, wdst + (s % NWS) * WSTG);
      if constexpr (o == 1) { dma16s_q(wo[1], wsrc, wdst + (s % NWS) * WSTG + 1024); wsrc += winc; }
    }
  };
  sfor<0, 4>([&](auto O) { issue_x(IC<0>{}, O); });
  issue_w(IC<0>{}, IC<0>{}); issue_w(IC<0>{}, IC<1>{});
  BAR();
  issue_w(IC<1>{}, IC<0>{}); issue_w(IC<1>{}, IC<1>{});
  const int gw = b * 8 + w;
  dma4(warm_addr(gw * 11 + min(l, 10), 0, x_, wqkv_, wg_, wu_), lb + L_DUMMY);
  const unsigned tag = ((unsigned)llvm_dispatch_id() + 1u) * 0x9E3779B1u ^ 0x5BD1E995u;
  u32x4 xr0, xr1;
  {
    const char* xrow = uptr((const char*)x_ + (size_t)gw * 1280);
    gload16a(xr0, l * 16, xrow);
    gload16a(xr1, 1024 + (l & 15) * 16, xrow);
  }
  const int fr1 = gw + 2048 < 2880 ? gw + 2048 : gw;
  const int wsel = w < 5 ? w : w - 5;
  auto side_dma = [&]() {
#pragma unroll
    for (int i = 0; i < 2; ++i) {
      const int fr = i ? fr1 : gw;
      const int fg = fr / 360, rem = fr - fg * 360, ks = rem / 5, nt = rem - ks * 5;
      const int slot = l >> 1, g_ = slot >> 3, j = slot & 7;
      const int u = (ks - 48) / 3, s_ = (ks - 48) - u * 3;
      const int id = min(8 * s_ + j, 19), e = id / 5, v = id - e * 5;
      const int d = v < 4 ? 4 * g_ + v : 16 + g_;
      const int row = ks < 48 ? 32 * ks + slot : (4 * u + e) * 20 + d;
      const char* pb_ = uptr(ks < 48 ? (const char*)wd_ : (const char*)wo_);
      dma16s(row * 1280 + (80 * fg + 16 * nt + 8 * (l & 1)) * 2, pb_, lb + L_PT + (w * 2 + i) * 1024);
    }
    dma16s(ga * 3200 + min((w & 3) * 64 + l, 199) * 16, (const char*)wphi_, lb + L_WPHI + (w & 3) * 1024);
  };

  int tok = l & 15, g = l >> 4, q = (l & 15) >> 2, p = l & 3;
  auto relane = [&]() { tid = opaque(threadIdx.x); l = tid & 63; tok = l & 15; g = l >> 4; q = (l & 15) >> 2; p = l & 3; };
  const int ab = opaque(L_WR + fgi * WREG + (p >> 1) * SUB + (16 * (g >> 1) + 4 * (g & 1) + q) * 80 + 40 * (p & 1));
  const int cb = opaque(ab + 8 * tq);
  int bb[2][2][2];
#pragma unroll
  for (int h = 0; h < 2; ++h)
#pragma unroll
    for (int e = 0; e < 2; ++e)
#pragma unroll
      for (int i = 0; i < 2; ++i)
        bb[h][e][i] = opaque(L_XR + (64 * tq + 32 * (i ^ fgi) + tok) * 128 + 16 * ((4 * e + g) ^ (tok & 7)) + h * 2 * XSTG);

  f32x4 acc[5][4];
#pragma unroll
  for (int t = 0; t < 5; ++t)
#pragma unroll
    for (int i = 0; i < 4; ++i) acc[t][i] = f32x4{0.f, 0.f, 0.f, 0.f};
  f32x4 csa = f32x4{0.f, 0.f, 0.f, 0.f}, csb = f32x4{0.f, 0.f, 0.f, 0.f};
  u32x4 ONES4 = u32x4{0x3F803F80u, 0x3F803F80u, 0x3F803F80u, 0x3F803F80u};
  asm volatile("" : "+v"(ONES4));
#pragma unroll
  for (int t = 0; t < 5; ++t)
#pragma unroll
    for (int i = 0; i < 4; ++i) pin4(acc[t][i]);
  pin4(csa); pin4(csb);
  const __amdgpu_buffer_rsrc_t rsw = __builtin_amdgcn_make_buffer_rsrc((void*)ws, 0, (int)WS_BYTES, 0x00020000);
  u32x4 stv[4], fc[2];
  auto ld_shared = [&]() {
#pragma unroll
    for (int i = 0; i < 4; ++i)
      stv[i] = __builtin_amdgcn_raw_buffer_load_b128(rsw, (tg * 256 + 64 * tq + 16 * ((i + 2 * fgi) & 3) + tok) * 16, (int)OFF_ST, 16);
  };
  auto sh_bad = [&]() -> bool {
    bool bad = false;
#pragma unroll
    for (int i = 0; i < 4; ++i) bad = bad || stv[i][1] != tag || stv[i][3] != tag;
    return __any(bad);
  };

  u32x4 fa[2][5], fb[2][4];
  auto readA = [&](auto S, auto T) {
    constexpr int s = decltype(S)::value, t = decltype(T)::value;
    fa[s & 1][t] = trread2(L + ab + (s % NWS) * WSTG + 8 * t, 640);
  };
  auto readB = [&](auto S, auto I) {
    constexpr int s = decltype(S)::value, i = decltype(I)::value;
    constexpr int sl = (s / 2) % NXS, h = sl >= 2 ? 1 : 0;
    fb[s & 1][i] = *(LAS u32x4*)(L + bb[h][s & 1][i >> 1] + (sl - 2 * h) * XSTG + (i & 1) * 2048);
  };

  sfor<0, 4>([&](auto O) { issue_x(IC<1>{}, O); });
  issue_w(IC<2>{}, IC<0>{}); issue_w(IC<2>{}, IC<1>{});
  dma4(warm_addr(gw * 11 + min(l, 10), 1, x_, wqkv_, wg_, wu_), lb + L_DUMMY);
  STAMP(0, 1);
  WAIT_V(pend_at(-1));
  BAR();
  STAMP(0, 2);
  sfor<0, 4>([&](auto I) { readB(IC<0>{}, I); });
  sfor<0, 5>([&](auto T) { readA(IC<0>{}, T); });
  fc[0] = trread2(L + cb, 640);
  if (tid < 64) *(LAS u32x4*)(L + L_ZERO + tid * 16) = u32x4{0, 0, 0, 0};
  SCHED();

  sfor<0, 20>([&](auto S) {
    constexpr int s = decltype(S)::value;
    if constexpr (s + 1 < 20) {
      constexpr int pend = pend_at(s);
      asm volatile("s_waitcnt vmcnt(%0) lgkmcnt(0)" ::"i"(pend) : "memory");
      BAR();
    }
    sfor<0, 5>([&](auto T) {
      constexpr int t = decltype(T)::value;
      sfor<0, 4>([&](auto I) {
        constexpr int i = decltype(I)::value;
        mfma16a(acc[t][i], fa[s & 1][t], fb[s & 1][i]);
        if constexpr (s + 1 < 20) {
          if constexpr (t == 0) readB(IC<s + 1>{}, I);
          if constexpr (i == 1) readA(IC<s + 1>{}, T);
        }
        if constexpr (i == 3 && t < 2) issue_x(IC<s / 2 + 2>{}, IC<2 * (s & 1) + t>{});
        if constexpr (i == 3 && (t == 2 || t == 3)) issue_w(IC<s + 3>{}, IC<t - 2>{});
        if constexpr (t == 4 && i == 0 && s + 1 < 20) fc[(s + 1) & 1] = trread2(L + cb + ((s + 1) % NWS) * WSTG, 640);
        if constexpr (t == 2 && i == 1) mfma16a(csa, fc[s & 1], ONES4);
        SCHED();
      });
    });
    if ((s & 3) == tq) mfma16a(csb, fa[s & 1][4], ONES4);
    if constexpr (s == 1) {
      relane();
      pinu(xr0); pinu(xr1);
      float v[16];
#pragma unroll
      for (int j = 0; j < 4; ++j) {
        v[2 * j] = __uint_as_float(xr0[j] << 16); v[2 * j + 1] = __uint_as_float(xr0[j] & 0xffff0000u);
        v[8 + 2 * j] = __uint_as_float(xr1[j] << 16); v[9 + 2 * j] = __uint_as_float(xr1[j] & 0xffff0000u);
      }
      const bool ex = l < 16;
      float a = 0.f, c = 0.f;
#pragma unroll
      for (int j = 0; j < 8; ++j) { a += v[j]; c += v[8 + j]; }
      const float mean = wave_sum(a + (ex ? c : 0.f)) * (1.f / 640.f);
      a = 0.f; c = 0.f;
#pragma unroll
      for (int j = 0; j < 8; ++j) { const float d0 = v[j] - mean, d1 = v[8 + j] - mean; a = fmaf(d0, d0, a); c = fmaf(d1, d1, c); }
      const float var = wave_sum(a + (ex ? c : 0.f)) * (1.f / 640.f);
      gst(ws + OFF_ST + (size_t)gw * 16, u32x4{__float_as_uint(-mean), tag, __float_as_uint(var + 1e-5f), tag});
    }
    if constexpr (s == 0) { relane(); dma4(warm_addr(gw * 11 + min(l, 10), 2, x_, wqkv_, wg_, wu_), lb + L_DUMMY); }
    if constexpr (s == 1) { relane(); dma4(warm_addr2(gw * 11 + min(l, 10), wd_, wo_), lb + L_DUMMY); }
    if constexpr (s == S_SIDE) { relane(); side_dma(); ld_shared(); }
#define ST_A 4
#define ST_B 9
#define ST_C 14
    if constexpr (s == ST_A) STAMP(0, 3);
    if constexpr (s == ST_B) STAMP(0, 4);
    if constexpr (s == ST_C) STAMP(0, 5);
    SCHED();
  });
  asm volatile("s_nop 7\n\ts_nop 7" ::: "memory");
  SCHED();
  STAMP(0, 6);
  relane();
  if (b < 210 && tid < 64) *(unsigned*)(ws + OFF_KVT + b * 256 + tid * 4) = 0xFFFFFFFFu;

  WAIT_V(0);
#pragma unroll
  for (int i = 0; i < 2; ++i) {
    const u32x4 v = trread2(L + L_PT + (w * 2 + i) * 1024 + (8 * g + q) * 32 + 8 * p, 128);
    gst_wt(ws + OFF_WT + (size_t)(i ? fr1 : gw) * 1024 + l * 16, v);
  }
  if (__builtin_expect(sh_bad(), 0)) {
    unsigned spin = 0;
    do {
      __builtin_amdgcn_s_sleep(4);
      ld_shared();
    } while (sh_bad() && ++spin < (1u << 16));
  }
  if (tok == 0) {
    *(LAS f32x4*)(L + L_CSS + (fgi * 128 + tq * 16 + 4 * g) * 4) = csa;
    *(LAS f32x4*)(L + L_CSS + (fgi * 128 + 64 + tq * 16 + 4 * g) * 4) = csb;
  }
  __syncthreads();
  float nmean[4], vare[4], rstd[4];
#pragma unroll
  for (int i = 0; i < 4; ++i) {
    nmean[i] = __uint_as_float(stv[i][0]); vare[i] = __uint_as_float(stv[i][2]);
    rstd[i] = __builtin_amdgcn_rsqf(vare[i]);
  }
#pragma unroll
  for (int t = 0; t < 5; ++t) {
    f32x4 c4 = *(LAS f32x4*)(L + L_CSS + (fgi * 128 + t * 16 + 4 * g) * 4);
    if (t == 4) {
#pragma unroll
      for (int k = 1; k < 4; ++k) c4 += *(LAS f32x4*)(L + L_CSS + (fgi * 128 + 64 + k * 16 + 4 * g) * 4);
    }
#pragma unroll
    for (int i = 0; i < 4; ++i)
#pragma unroll
      for (int c = 0; c < 4; ++c) acc[t][i][c] = fmaf(nmean[i], c4[c], acc[t][i][c]);
  }
  STAMP(0, 7);
  const int tile0 = tg * 16 + tq * 4;

  if (kind == 3) {
    const int hi = l >> 5, pp = g & 1;
#pragma unroll
    for (int i = 0; i < 2; ++i) {
      const float rs_ = hi ? rstd[i + 2] : rstd[i], ve = hi ? vare[i + 2] : vare[i];
      const float c1 = -2.0f * 0.7978845608028654f * 1.4426950408889634f;
      const float cA = c1 * rs_, cB = c1 * 0.044715f * rs_ * rs_ * rs_;
      unsigned hp[10];
#pragma unroll
      for (int t = 0; t < 5; ++t) {
#pragma unroll
        for (int c2 = 0; c2 < 2; ++c2) {
          f32x2 gg, uu;
#pragma unroll
          for (int c = 0; c < 2; ++c) {
            const float va = acc[t][i][2 * c2 + c], vb = acc[t][i + 2][2 * c2 + c];
            const auto sw = __builtin_amdgcn_permlane32_swap(__float_as_uint(va), __float_as_uint(vb), false, false);
            const unsigned s0 = sw[0], s1 = sw[1];
            gg[c] = __uint_as_float(s0); uu[c] = __uint_as_float(s1);
          }
          const f32x2 arg = __builtin_elementwise_fma(gg * gg, f32x2{cB, cB}, f32x2{cA, cA}) * gg;
          const f32x2 e = f32x2{__builtin_amdgcn_exp2f(arg[0]), __builtin_amdgcn_exp2f(arg[1])};
          const f32x2 dn = __builtin_elementwise_fma(e, f32x2{ve, ve}, f32x2{ve, ve});
          const f32x2 rr = f32x2{__builtin_amdgcn_rcpf(dn[0]), __builtin_amdgcn_rcpf(dn[1])};
          const f32x2 hv = gg * uu * rr;
          hp[2 * t + c2] = pk2(hv[0], hv[1]);
        }
      }
      const int tl = tile0 + ((i + 2 * hi + 2 * fgi) & 3);
      char* dst = ws + OFF_H + (size_t)tl * 49152 + (mbase >> 3) * 256 + tok * 16;
      if (pp == 0) {
        gst(dst, u32x4{hp[0], hp[1], hp[2], hp[3]});
        gst(dst + 256, u32x4{hp[4], hp[5], hp[6], hp[7]});
        gst(dst + 512, u32x2{hp[8], hp[9]});
      } else {
        gst(dst + 520, u32x2{hp[0], hp[1]});
        gst(dst + 768, u32x4{hp[2], hp[3], hp[4], hp[5]});
        gst(dst + 1024, u32x4{hp[6], hp[7], hp[8], hp[9]});
      }
    }
  } else if (kind == 2) {
#pragma unroll
    for (int i = 0; i < 4; ++i) {
      const int trow = 64 * tq + 16 * ((i + 2) & 3) + tok;
#pragma unroll
      for (int t = 0; t < 5; ++t)
        *(LAS u32x2*)(L + L_V + trow * V_PITCH + 40 * g + 8 * t) =
            u32x2{pk2(acc[t][i][0] * rstd[i], acc[t][i][1] * rstd[i]), pk2(acc[t][i][2] * rstd[i], acc[t][i][3] * rstd[i])};
      if (g == 0) *(LAS u32x2*)(L + L_V + trow * V_PITCH + 160) = u32x2{0x00003F80u, 0u};
    }
  } else {
    u32x4 bq[4][3];
#pragma unroll
    for (int i = 0; i < 4; ++i) {
#pragma unroll
      for (int s = 0; s < 2; ++s)
        bq[i][s] = u32x4{pk2(acc[2 * s][i][0], acc[2 * s][i][1]), pk2(acc[2 * s][i][2], acc[2 * s][i][3]),
                         pk2(acc[2 * s + 1][i][0], acc[2 * s + 1][i][1]), pk2(acc[2 * s + 1][i][2], acc[2 * s + 1][i][3])};
      bq[i][2] = u32x4{pk2(acc[4][i][0], acc[4][i][1]), pk2(acc[4][i][2], acc[4][i][3]), 0u, 0u};
    }
    const int wb = (p == g) ? (L_WPHI + g * 800 + q * 40) : L_ZERO;
    f32x4 pq[5][4];
#pragma unroll
    for (int tt = 0; tt < 5; ++tt) {
      const u32x4 a0 = trread2(L + wb + 8 * tt, 160), a1 = trread2(L + wb + 8 * tt + 320, 160);
      const u32x2 a2l = trread(L + wb + 8 * tt + 640);
      const u32x4 a2 = u32x4{a2l[0], a2l[1], 0u, 0u};
#pragma unroll
      for (int i = 0; i < 4; ++i) {
        f32x4 o = f32x4{0.f, 0.f, 0.f, 0.f};
        o = mfma16(a0, bq[i][0], o);
        o = mfma16(a1, bq[i][1], o);
        o = mfma16(a2, bq[i][2], o);
        pq[tt][i] = o;
      }
    }
#pragma unroll
    for (int i = 0; i < 4; ++i) {
      const float c1 = rstd[i] * 1.4426950408889634f;
      float mx = pq[0][i][0], mn = pq[0][i][0];
#pragma unroll
      for (int f = 1; f < 20; ++f) { mx = fmaxf(mx, pq[f >> 2][i][f & 3]); mn = fminf(mn, pq[f >> 2][i][f & 3]); }
      const float bx = -mx * c1, bn = mn * c1;
      f32x2 ep[10], en[10], sp2 = f32x2{0.f, 0.f}, sn2 = f32x2{0.f, 0.f};
#pragma unroll
      for (int f = 0; f < 10; ++f) {
        const f32x2 pv = f32x2{pq[f >> 1][i][2 * (f & 1)], pq[f >> 1][i][2 * (f & 1) + 1]};
        const f32x2 ap = __builtin_elementwise_fma(pv, f32x2{c1, c1}, f32x2{bx, bx});
        const f32x2 an = __builtin_elementwise_fma(pv, f32x2{-c1, -c1}, f32x2{bn, bn});
        ep[f] = f32x2{__builtin_amdgcn_exp2f(ap[0]), __builtin_amdgcn_exp2f(ap[1])};
        en[f] = f32x2{__builtin_amdgcn_exp2f(an[0]), __builtin_amdgcn_exp2f(an[1])};
        sp2 += ep[f]; sn2 += en[f];
      }
      const float rp = __builtin_amdgcn_rcpf(sp2[0] + sp2[1]), rn = __builtin_amdgcn_rcpf(sn2[0] + sn2[1]);
      unsigned o[20];
#pragma unroll
      for (int f = 0; f < 10; ++f) {
        const f32x2 a = ep[f] * f32x2{rp, rp}, c = en[f] * f32x2{rn, rn};
        o[f] = pk2(a[0], a[1]);
        o[10 + f] = pk2(c[0], c[1]);
      }
      if (kind == 0) {
        char* dst = ws + OFF_PQ + (size_t)((tile0 + i) * 8 + ga) * 5120 + tok * 16;
#pragma unroll
        for (int ci = 0; ci < 4; ++ci) gst(dst + g * 1024 + ci * 256, u32x4{o[4 * ci], o[4 * ci + 1], o[4 * ci + 2], o[4 * ci + 3]});
        gst(dst + 4096 + g * 256, u32x4{o[16], o[17], o[18], o[19]});
      } else {
        const int trow = 64 * tq + 16 * i + tok;
#pragma unroll
        for (int ci = 0; ci < 5; ++ci)
          *(LAS u32x4*)(L + L_PK + trow * PK_PITCH + g * 80 + ci * 16) = u32x4{o[4 * ci], o[4 * ci + 1], o[4 * ci + 2], o[4 * ci + 3]};
      }
    }
  }
  if (bt == 0) {
    __syncthreads();
    const int e = w >> 1, bh = w & 1;
    const int rowoff = 8 * g + q;
    LAS char* pa = L + L_PK + rowoff * PK_PITCH + e * 80 + 8 * p;
    LAS char* pa2 = L + L_PK + rowoff * PK_PITCH + e * 80 + 64 + 8 * (p & 1);
    LAS char* pb = L + L_V + rowoff * V_PITCH + (bh == 0 ? e * 40 + 8 * p : (p == 0 ? e * 40 + 32 : 160));
    f32x4 d[3];
#pragma unroll
    for (int k = 0; k < 3; ++k) d[k] = f32x4{0.f, 0.f, 0.f, 0.f};
#pragma unroll
    for (int ks = 0; ks < 8; ++ks) {
      const u32x4 bb = trread2(pb + ks * 32 * V_PITCH, 4 * V_PITCH);
      d[0] = mfma16(trread2(pa + ks * 32 * PK_PITCH, 4 * PK_PITCH), bb, d[0]);
      d[1] = mfma16(trread2(pa + ks * 32 * PK_PITCH + 32, 4 * PK_PITCH), bb, d[1]);
      d[2] = mfma16(trread2(pa2 + ks * 32 * PK_PITCH, 4 * PK_PITCH), bb, d[2]);
    }
    const int dcol = 16 * bh + tok;
    if (dcol <= 20) {
      float* dst = (float*)(ws + OFF_KVP) + ((size_t)(tg * 32 + 4 * ga + e) * 21 + dcol) * 40 + 4 * g;
      *(f32x4*)(dst) = d[0];
      *(f32x4*)(dst + 16) = d[1];
      if (g < 2) *(f32x4*)(dst + 32) = d[2];
    }
  }
  asm volatile("" ::"v"(cpf));
  STAMP(0, 8);
  WAIT_V(0);
  STAMP_FLUSH(0);
}

constexpr int K2_LDS = 163840;
__global__ __launch_bounds__(512, 2) void k2_out(const __hip_bfloat16* __restrict__ x_, char* __restrict__ ws,
                                                 __hip_bfloat16* __restrict__ out_) {
  extern __shared__ __attribute__((aligned(16))) char smem[];
  LAS char* L = (LAS char*)smem;
  const int tid = threadIdx.x, l = tid & 63;
  const int w = __builtin_amdgcn_readfirstlane(tid >> 6);
  const int b = blockIdx.x, xcd = b & 7, idx = b >> 3;
  STAMP_DECL;
  STAMP(1, 0);
  const __amdgpu_buffer_rsrc_t rs = __builtin_amdgcn_make_buffer_rsrc((void*)ws, 0, (int)WS_BYTES, 0x00020000);
  const bool red = (w == 0 && b < 210);
  f32x2 pv[NP];
  if (red) {
    const GAS f32x2* pp = (const GAS f32x2*)(ws + OFF_KVP) + b * 64 + l;
#pragma unroll
    for (int k = 0; k < NP; ++k) pv[k] = pp[k * 13440];
  }
  SCHED();
  BAR();
  const unsigned cpf = code_prefetch<K2_PF>(idx, tid);
  const int tg = xcd * 4 + (idx & 3);
  const int fg = idx >> 2;
  const int vo = l * 16;
  const int so_h = (int)OFF_H + tg * 4 * 49152 + 6 * w * 1024;
  const int so_a = (int)OFF_WT + fg * 72 * 5120 + 6 * w * 5120;
  const int so_p = (int)OFF_PQ + (tg * 32 + w) * 5120;
  const int so_o = (int)OFF_WT + fg * 72 * 5120 + (48 + 3 * w) * 5120;
  auto ldg = [&](int so) -> u32x4 { return __builtin_amdgcn_raw_buffer_load_b128(rs, vo, so, 0); };
  f32x4 acc[5][4];
#pragma unroll
  for (int nt = 0; nt < 5; ++nt)
#pragma unroll
    for (int i = 0; i < 4; ++i) { acc[nt][i] = f32x4{0.f, 0.f, 0.f, 0.f}; pin4(acc[nt][i]); }

  u32x4 fa[6][5], fb[6][4], pf[4][5], ca[3][5];
  auto ld = [&](auto K) {
    constexpr int k = decltype(K)::value;
#pragma unroll
    for (int i = 0; i < 4; ++i) fb[k][i] = ldg(so_h + i * 49152 + k * 1024);
#pragma unroll
    for (int nt = 0; nt < 5; ++nt) fa[k][nt] = ldg(so_a + k * 5120 + nt * 1024);
    SCHED();
  };
  auto mm = [&](auto K) {
    constexpr int k = decltype(K)::value;
#pragma unroll
    for (int nt = 0; nt < 5; ++nt)
#pragma unroll
      for (int i = 0; i < 4; ++i) {
        mfma16a(acc[nt][i], fa[k][nt], fb[k][i]);
      }
    SCHED();
  };
  auto ldp = [&](int hp) {
#pragma unroll
    for (int i = 0; i < 4; ++i) {
      pf[i][2 * hp] = ldg(so_p + i * 40960 + 2 * hp * 1024);
      pf[i][2 * hp + 1] = ldg(so_p + i * 40960 + (2 * hp + 1) * 1024);
      if (hp == 0) pf[i][4] = ldg(so_p + i * 40960 + 4096);
    }
    SCHED();
  };
  auto ldo = [&](int s) {
#pragma unroll
    for (int nt = 0; nt < 5; ++nt) ca[s][nt] = ldg(so_o + s * 5120 + nt * 1024);
    SCHED();
  };
  const int r = l & 15, g = l >> 4;
  const int row1 = (r & 3) == 0 ? 16 + (r >> 2) : 20;
  const int so_k = (int)OFF_KVT + 4 * w * 1680;
  const int ko0 = r * 80 + g * 16, ko1 = row1 * 80 + g * 16, kt0 = r * 80 + g * 1680 + 64, kt1 = row1 * 80 + g * 1680 + 64;
  u32x4 ka[4][2], kt[2];
  auto ldk = [&](int hp) {
#pragma unroll
    for (int hd = 2 * hp; hd < 2 * hp + 2; ++hd) {
      ka[hd][0] = __builtin_amdgcn_raw_buffer_load_b128(rs, ko0, so_k + hd * 1680, 16);
      ka[hd][1] = __builtin_amdgcn_raw_buffer_load_b128(rs, ko1, so_k + hd * 1680, 16);
    }
    if (hp == 0) {
      kt[0] = __builtin_amdgcn_raw_buffer_load_b128(rs, kt0, so_k, 16);
      kt[1] = __builtin_amdgcn_raw_buffer_load_b128(rs, kt1, so_k, 16);
    }
  };
  auto kbad = [&](int hp) -> bool {
    unsigned mxw = 0u;
#pragma unroll
    for (int k = 0; k < 2; ++k)
#pragma unroll
      for (int j = 0; j < 4; ++j) {
        if (hp == 0) mxw = max(mxw, kt[k][j]);
#pragma unroll
        for (int hd = 2 * hp; hd < 2 * hp + 2; ++hd) mxw = max(mxw, ka[hd][k][j]);
      }
    return __any(mxw == 0xFFFFFFFFu);
  };

  if (red) {
    f32x2 s = pv[0];
#pragma unroll
    for (int k = 1; k < NP; ++k) s += pv[k];
    __builtin_amdgcn_raw_buffer_store_b32(pk2(s[0], s[1]), rs, (int)OFF_KVT + b * 256 + l * 4, 0, 16);
  }
  SCHED();
  STAMP(1, 1);
  ld(IC<0>{}); ld(IC<1>{}); ld(IC<2>{});
  mm(IC<0>{}); ld(IC<3>{});
  asm volatile("" ::"v"(cpf));
  mm(IC<1>{}); ldp(0);
  mm(IC<2>{}); ldk(0); SCHED();
  mm(IC<3>{}); ldk(1); ldp(1);
  STAMP(1, 2);
  unsigned op[4][12];
#pragma unroll
  for (int hp = 0; hp < 2; ++hp) {
    if (__builtin_expect(kbad(hp), 0)) {
      unsigned spin = 0;
      do {
        __builtin_amdgcn_s_sleep(4);
        ldk(hp);
      } while (kbad(hp) && ++spin < (1u << 14));
    }
    if (hp == 0) STAMP(1, 3);
    u32x4 km[2][2];
#pragma unroll
    for (int z = 0; z < 2; ++z)
#pragma unroll
      for (int k = 0; k < 2; ++k) km[z][k] = (g == 2 * hp + z) ? kt[k] : u32x4{0u, 0u, 0u, 0u};
#pragma unroll
    for (int i = 0; i < 4; ++i) {
      float ov[10];
#pragma unroll
      for (int z = 0; z < 2; ++z) {
        const int hd = 2 * hp + z;
        f32x4 o0 = f32x4{0.f, 0.f, 0.f, 0.f}, o1 = f32x4{0.f, 0.f, 0.f, 0.f};
        o0 = mfma16(ka[hd][0], pf[i][hd], o0);
        o1 = mfma16(ka[hd][1], pf[i][hd], o1);
        o0 = mfma16(km[z][0], pf[i][4], o0);
        o1 = mfma16(km[z][1], pf[i][4], o1);
        const float inv = __builtin_amdgcn_rcpf(o1[1] + 1e-6f);
#pragma unroll
        for (int v = 0; v < 4; ++v) ov[5 * z + v] = o0[v] * inv;
        ov[5 * z + 4] = o1[0] * inv;
      }
#pragma unroll
      for (int j = 0; j < 5; ++j) op[i][5 * hp + j] = (unsigned)opaque((int)pk2(ov[2 * j], ov[2 * j + 1]));
      op[i][10] = 0u; op[i][11] = 0u;
      if (i & 1) SCHED();
    }
    SCHED();
    if (hp == 0) ld(IC<4>{});
    else { ld(IC<5>{}); ldo(0); }
  }
  u32x4 ob[3][4];
#pragma unroll
  for (int s = 0; s < 3; ++s)
#pragma unroll
    for (int i = 0; i < 4; ++i) { ob[s][i] = u32x4{op[i][4 * s], op[i][4 * s + 1], op[i][4 * s + 2], op[i][4 * s + 3]}; pinu(ob[s][i]); }
  SCHED();
  mm(IC<4>{}); ldo(1); ldo(2);
  u32x2 xv[3];
#pragma unroll
  for (int k = 0; k < 3; ++k) {
    const int t = min(w + 8 * k, 19), nt = t >> 2, i = t & 3;
    xv[k] = *(const u32x2*)((const char*)x_ + ((size_t)(tg * 64 + i * 16 + (l & 15)) * 640 + fg * 80 + nt * 16 + 4 * (l >> 4)) * 2);
  }
  SCHED();
  mm(IC<5>{});
#pragma unroll
  for (int s = 0; s < 3; ++s) {
#pragma unroll
    for (int nt = 0; nt < 5; ++nt)
#pragma unroll
      for (int i = 0; i < 4; ++i) mfma16a(acc[nt][i], ca[s][nt], ob[s][i]);
  }
  SCHED();
  asm volatile("s_nop 7\n\ts_nop 7" ::: "memory");
  SCHED();
  STAMP(1, 4);
#pragma unroll
  for (int nt = 0; nt < 5; ++nt)
#pragma unroll
    for (int i = 0; i < 4; ++i) *(LAS f32x4*)(L + ((w * 20 + nt * 4 + i) * 64 + l) * 16) = acc[nt][i];
  __syncthreads();
  STAMP(1, 5);
#pragma unroll
  for (int k = 0; k < 3; ++k) {
    const int t = w + 8 * k;
    if (t < 20) {
      f32x4 s = *(LAS f32x4*)(L + (t * 64 + l) * 16);
#pragma unroll
      for (int ww = 1; ww < 8; ++ww) s += *(LAS f32x4*)(L + ((ww * 20 + t) * 64 + l) * 16);
      const int nt = t >> 2, i = t & 3;
      const size_t off = ((size_t)(tg * 64 + i * 16 + (l & 15)) * 640 + fg * 80 + nt * 16 + 4 * (l >> 4)) * 2;
      const float x0 = __builtin_bit_cast(float, xv[k][0] << 16), x1 = __builtin_bit_cast(float, xv[k][0] & 0xffff0000u);
      const float x2 = __builtin_bit_cast(float, xv[k][1] << 16), x3 = __builtin_bit_cast(float, xv[k][1] & 0xffff0000u);
      *(u32x2*)((char*)out_ + off) = u32x2{pk2(s[0] + x0, s[1] + x1), pk2(s[2] + x2, s[3] + x3)};
    }
  }
  STAMP(1, 6);
  STAMP_FLUSH(1);
}

extern "C" void kernel_launch(void* const* d_in, const int* in_sizes, int n_in, void* d_out, int out_size, void* d_ws,
                              size_t ws_size, hipStream_t stream) {
  const __hip_bfloat16* x = (const __hip_bfloat16*)d_in[0];
  const __hip_bfloat16* wqkv = (const __hip_bfloat16*)d_in[1];
  const __hip_bfloat16* wphi = (const __hip_bfloat16*)d_in[2];
  const __hip_bfloat16* wo = (const __hip_bfloat16*)d_in[3];
  const __hip_bfloat16* wg = (const __hip_bfloat16*)d_in[5];
  const __hip_bfloat16* wu = (const __hip_bfloat16*)d_in[6];
  const __hip_bfloat16* wd = (const __hip_bfloat16*)d_in[7];
  (void)hipFuncSetAttribute((const void*)k1_proj, hipFuncAttributeMaxDynamicSharedMemorySize, K1_LDS);
  (void)hipFuncSetAttribute((const void*)k2_out, hipFuncAttributeMaxDynamicSharedMemorySize, K2_LDS);
  k1_proj<<<256, 512, K1_LDS, stream>>>(x, wqkv, wphi, wo, wg, wu, wd, (char*)d_ws);
  k2_out<<<256, 512, K2_LDS, stream>>>(x, (char*)d_ws, (__hip_bfloat16*)d_out);
}
```

```cpp
#define SC1ST
#include <hip/hip_runtime.h>
#include <hip/hip_bf16.h>
#include <stdint.h>

#define GAS __attribute__((address_space(1)))
#define LAS __attribute__((address_space(3)))
#define DI __device__ __forceinline__
typedef __bf16 bf16x8 __attribute__((ext_vector_type(8)));
typedef __bf16 bf16x2 __attribute__((ext_vector_type(2)));
typedef short s16x4 __attribute__((ext_vector_type(4)));
typedef float f32x4 __attribute__((ext_vector_type(4)));
typedef float f32x2 __attribute__((ext_vector_type(2)));
typedef unsigned u32x4 __attribute__((ext_vector_type(4)));
typedef unsigned u32x2 __attribute__((ext_vector_type(2)));

extern "C" __device__ unsigned long long llvm_dispatch_id() __asm("llvm.amdgcn.dispatch.id");
template <int N> struct IC { static constexpr int value = N; };
template <int B, int E, class F> DI void sfor(F&& f) {
  if constexpr (B < E) { f(IC<B>{}); sfor<B + 1, E>(f); }
}

constexpr unsigned OFF_H = 0;
constexpr unsigned OFF_PQ = 6291456;
constexpr unsigned OFF_WT = 11534336;
constexpr unsigned OFF_KVP = 14483456;
constexpr unsigned OFF_KVT = 15343616;
constexpr unsigned OFF_ST = 15397376;
constexpr unsigned OFF_O = OFF_ST + 32768;
constexpr unsigned WS_BYTES = OFF_O + 2621440;
constexpr int NP = 8;

constexpr int SUB = 2688;
constexpr int WREG = SUB + 2560;
constexpr int WSTG = 2 * WREG;
constexpr int XSTG = 32768;
constexpr int NXS = 3, NWS = 4;
constexpr int L_WPHI = 0;
constexpr int L_ZERO = L_WPHI + 4096;
constexpr int L_ST = L_ZERO + 1024;
constexpr int L_CSS = L_ST + 2048;
constexpr int L_DUMMY = L_CSS + 2048;
constexpr int L_RING = L_CSS + 2560;
constexpr int L_XR = L_RING, L_WR = L_XR + NXS * XSTG;
constexpr int L_PT = L_XR + XSTG;
constexpr int K1_LDS = L_WR + NWS * WSTG;
constexpr int PK_PITCH = 336, V_PITCH = 176;
constexpr int L_PK = L_RING, L_V = L_PK + 256 * PK_PITCH;

DI unsigned pk2(float a, float b) {
  bf16x2 v; v[0] = (__bf16)a; v[1] = (__bf16)b;
  return __builtin_bit_cast(unsigned, v);
}
DI float dot2(unsigned a, unsigned b, float c) {
  return __builtin_amdgcn_fdot2_f32_bf16(__builtin_bit_cast(bf16x2, a), __builtin_bit_cast(bf16x2, b), c, false);
}
DI u32x2 trread(LAS char* p) {
  s16x4 v = __builtin_amdgcn_ds_read_tr16_b64_v4i16((LAS s16x4*)p);
  return __builtin_bit_cast(u32x2, v);
}
DI u32x4 trread2(LAS char* p, int hi) {
  u32x2 a = trread(p), b = trread(p + hi);
  return u32x4{a[0], a[1], b[0], b[1]};
}
DI f32x4 mfma16(u32x4 a, u32x4 b, f32x4 c) {
  return __builtin_amdgcn_mfma_f32_16x16x32_bf16(__builtin_bit_cast(bf16x8, a), __builtin_bit_cast(bf16x8, b), c, 0, 0, 0);
}
DI void mfma16a(f32x4& c, const u32x4& a, const u32x4& b) {
  asm volatile("v_mfma_f32_16x16x32_bf16 %0, %1, %2, %0" : "+v"(c) : "v"(a), "v"(b));
}
DI void dma16(const void* g, int ldsaddr) {
  asm volatile("s_mov_b32 m0, %1\n\ts_nop 0\n\tglobal_load_lds_dwordx4 %0, off" ::"v"(g), "s"(ldsaddr) : "memory");
}
DI void gload16a(u32x4& dst, unsigned voff, const char* sbase) {
  asm volatile("global_load_dwordx4 %0, %1, %2" : "=v"(dst) : "v"(voff), "s"(sbase));
}
template <int CTRL, int RM> DI float dpp_add(float v) {
  return v + __int_as_float(__builtin_amdgcn_update_dpp(0, __float_as_int(v), CTRL, RM, 0xf, false));
}
DI float wave_sum(float v) {
  v = dpp_add<0x111, 0xf>(v);
  v = dpp_add<0x112, 0xf>(v);
  v = dpp_add<0x114, 0xf>(v);
  v = dpp_add<0x118, 0xf>(v);
  v = dpp_add<0x142, 0xa>(v);
  v = dpp_add<0x143, 0xc>(v);
  return __int_as_float(__builtin_amdgcn_readlane(__float_as_int(v), 63));
}
DI void dma4(const void* g, int ldsaddr) {
  asm volatile("s_mov_b32 m0, %1\n\ts_nop 0\n\tglobal_load_lds_dword %0, off" ::"v"(g), "s"(ldsaddr) : "memory");
}
template <int OFF = 0> DI void dma16s(unsigned voff, const char* sbase, int ldsaddr) {
  asm volatile("s_mov_b32 m0, %2\n\ts_nop 0\n\tglobal_load_lds_dwordx4 %0, %1 offset:%3" ::"v"(voff), "s"(sbase), "s"(ldsaddr - OFF), "i"(OFF) : "memory");
}
DI void dma16s_q(unsigned voff, const char* sbase, int ldsaddr) {
  asm volatile("s_mov_b32 m0, %2\n\ts_mov_b64 exec, 0xffff\n\tglobal_load_lds_dwordx4 %0, %1\n\ts_mov_b64 exec, -1" ::"v"(voff), "s"(sbase),
               "s"(ldsaddr)
               : "memory");
}
template <int C, int OFF = 0> DI void dma16s_if(int sel, unsigned voff, const char* sbase, int ldsaddr) {
  asm volatile("s_cmp_lg_u32 %3, %4\n\ts_cbranch_scc1 .Lskip%=\n\ts_mov_b32 m0, %2\n\ts_nop 0\n\tglobal_load_lds_dwordx4 %0, %1 offset:%5\n.Lskip%=:" ::"v"(voff),
               "s"(sbase), "s"(ldsaddr - OFF), "s"(sel), "i"(C), "i"(OFF)
               : "memory", "scc");
}
template <int C> DI void dma16s_q_if(int sel, unsigned voff, const char* sbase, int ldsaddr) {
  asm volatile("s_cmp_lg_u32 %3, %4\n\ts_cbranch_scc1 .Lskip%=\n\ts_mov_b32 m0, %2\n\ts_mov_b64 exec, 0xffff\n\tglobal_load_lds_dwordx4 %0, %1\n\ts_mov_b64 exec, -1\n.Lskip%=:" ::"v"(voff),
               "s"(sbase), "s"(ldsaddr), "s"(sel), "i"(C)
               : "memory", "scc");
}
DI int opaque(int v) { asm volatile("" : "+v"(v)); return v; }
DI void keep3(unsigned (&v)[3]) { asm volatile("" ::"v"(v[0]), "v"(v[1]), "v"(v[2])); }
DI void pin4(f32x4& v) { asm volatile("" : "+v"(v)); }
DI void pinu(u32x4& v) { asm volatile("" : "+v"(v)); }
DI void gst(char* p, u32x4 v) {
  asm volatile("global_store_dwordx4 %0, %1, off sc1\n\ts_nop 1" ::"v"(p), "v"(v) : "memory");
}
DI void gst_wt(char* p, u32x4 v) { asm volatile("global_store_dwordx4 %0, %1, off sc1\n\ts_nop 1" ::"v"(p), "v"(v) : "memory"); }
DI void gst(char* p, u32x2 v) {
  asm volatile("global_store_dwordx2 %0, %1, off sc1\n\ts_nop 1" ::"v"(p), "v"(v) : "memory");
}
DI void pinf(float& v) { asm volatile("" : "+v"(v)); }
#define WAIT_V(n) asm volatile("s_waitcnt vmcnt(%0)" ::"i"(n) : "memory")
#define STAMP_DECL
#define STAMP(k, i)
#define STAMP_FLUSH(k)
template <int NL> DI unsigned code_prefetch(int idx, int tid) {
  unsigned long long pc;
  asm volatile("s_getpc_b64 %0" : "=s"(pc));
  unsigned v = 0;
  if (tid < NL) v = *(const GAS unsigned*)(pc + (unsigned long long)(idx * NL + tid) * 64);
  return v;
}
DI const char* uptr(const char* p) {
  const unsigned long long v = (unsigned long long)p;
  const unsigned lo = __builtin_amdgcn_readfirstlane((unsigned)v), hi = __builtin_amdgcn_readfirstlane((unsigned)(v >> 32));
  return (const char*)(((unsigned long long)hi << 32) | lo);
}
#define BAR() __builtin_amdgcn_s_barrier()
#define SCHED() __builtin_amdgcn_sched_barrier(0)
#define K1_PF 15
#define K2_PF 3

#define S_WARM 40
#define S_PT 4
constexpr int NB = 1;
constexpr int S_SIDE = 16;
constexpr int xtra(int s) { return (s < 2 ? NB : 0) + (s == 1 ? 1 : 0) + (s == S_SIDE ? 1 + 4 : 0); }
constexpr int pend_at(int s) {
  int n = 0;
  auto op = [&](int cnt, int need) { if (need <= s + 1) n = 0; else n += cnt; };
  op(4, 0); op(2, 0); op(2, 1); op(NB + 2, 99); op(4, 2); op(2, 2); op(NB, 99);
  for (int st = 0; st < s; ++st) {
    if (st / 2 + 2 < 10) op(2, 2 * (st / 2 + 2));
    if (st + 3 < 20) op(2, st + 3);
    op(xtra(st), 99);
  }
  return n;
}
DI const char* warm_addr(int n, int j, const void* px, const void* pq, const void* pg, const void* pu) {
  n = min(n, 21119);
  const int xr = n / 3, xl = n - 3 * xr;
  const int m = max(n - 6144, 0), r = m / 78, c = m - 78 * r;
  const unsigned k = 64 + 192 * j + r;
  unsigned long long a = (unsigned long long)pq + k * 3840u + c * 128u;
  a = c >= 30 ? (unsigned long long)pg + k * 3072u + (c - 30) * 128u : a;
  a = c >= 54 ? (unsigned long long)pu + k * 3072u + (c - 54) * 128u : a;
  a = n < 6144 ? (unsigned long long)px + xr * 1280u + (1 + 3 * j + xl) * 128u : a;
  return (const char*)a;
}
DI const char* warm_addr2(int n, const void* pd, const void* po) {
  n = min(n, 21759);
  return (const char*)((n < 15360 ? (unsigned long long)pd : (unsigned long long)po - 15360ull * 128) + (unsigned long long)n * 128);
}

__global__ __launch_bounds__(512, 2) void k1_proj(const __hip_bfloat16* __restrict__ x_, const __hip_bfloat16* __restrict__ wqkv_,
                                                  const __hip_bfloat16* __restrict__ wphi_, const __hip_bfloat16* __restrict__ wo_,
                                                  const __hip_bfloat16* __restrict__ wg_, const __hip_bfloat16* __restrict__ wu_,
                                                  const __hip_bfloat16* __restrict__ wd_, char* __restrict__ ws) {
  extern __shared__ __attribute__((aligned(16))) char smem[];
  LAS char* L = (LAS char*)smem;
  const int lb = (int)(size_t)L;
  STAMP_DECL;
  STAMP(0, 0);
  int tid = threadIdx.x, l = tid & 63;
  const int w = __builtin_amdgcn_readfirstlane(tid >> 6);
  const int tq = w & 3, fgi = w >> 2;
  const int b = blockIdx.x, xcd = b & 7, idx = b >> 3;
  const unsigned cpf = code_prefetch<K1_PF>(idx, tid);
  const int tg = (xcd & 1) * 4 + (idx & 3);
  const int cg = (xcd >> 1) * 8 + (idx >> 2);
  const int ga = cg >> 2, bt = cg & 3;
  const int kind = bt == 0 ? 1 + fgi : ((bt == 1 && fgi == 0) ? 0 : 3);
  const int mj = bt == 1 ? ga : 8 + 4 * ga + 2 * (bt - 2) + fgi;
  const int mbase = min(40 * mj, 1496);

  const unsigned xo0 = (32 * w + (l >> 3)) * 1280 + 16 * ((l & 7) ^ ((l >> 3) & 7));
  const char* xs = uptr((const char*)x_ + (size_t)tg * 256 * 1280);
  const int wsub = tq >> 1;
  const int rs = kind == 3 ? 3072 : 3840;
  unsigned wo[2];
#pragma unroll
  for (int i = 0; i < 2; ++i) {
    const int c = min(64 * i + l, 79), pl = c / 5, ch = c - pl * 5;
    const int pos = 16 * (tq & 1) + pl;
    const int krow = (pos & 19) | ((pos & 4) << 1) | ((pos & 8) >> 1);
    wo[i] = krow * rs + 16 * ch;
  }
  const char* wsrc = uptr(kind == 3 ? (const char*)(wsub ? wu_ : wg_) + mbase * 2
                                    : (const char*)wqkv_ + (640 * kind + 80 * ga + 40 * wsub) * 2);
  const int winc = 32 * rs;
  const int xdst = lb + L_XR + w * 4096;
  const int wdst = lb + L_WR + fgi * WREG + wsub * SUB + (tq & 1) * 1280;
  auto issue_x = [&](auto J, auto O) {
    constexpr int j = decltype(J)::value, o = decltype(O)::value;
    if constexpr (j < 10) dma16s<j * 128>(xo0, xs + o * 10240, xdst + (j % NXS) * XSTG + o * 1024);
  };
  auto issue_w = [&](auto S, auto O) {
    constexpr int s = decltype(S)::value, o = decltype(O)::value;
    if constexpr (s < 20) {
      if constexpr (o == 0) dma16s(wo[0], wsrc, wdst + (s % NWS) * WSTG);
      if constexpr (o == 1) { dma16s_q(wo[1], wsrc, wdst + (s % NWS) * WSTG + 1024); wsrc += winc; }
    }
  };
  sfor<0, 4>([&](auto O) { issue_x(IC<0>{}, O); });
  issue_w(IC<0>{}, IC<0>{}); issue_w(IC<0>{}, IC<1>{});
  BAR();
  issue_w(IC<1>{}, IC<0>{}); issue_w(IC<1>{}, IC<1>{});
  const int gw = b * 8 + w;
  dma4(warm_addr(gw * 11 + min(l, 10), 0, x_, wqkv_, wg_, wu_), lb + L_DUMMY);
  const unsigned tag = ((unsigned)llvm_dispatch_id() + 1u) * 0x9E3779B1u ^ 0x5BD1E995u;
  u32x4 xr0, xr1;
  {
    const char* xrow = uptr((const char*)x_ + (size_t)gw * 1280);
    gload16a(xr0, l * 16, xrow);
    gload16a(xr1, 1024 + (l & 15) * 16, xrow);
  }
  const int mb = (ga * 2 + (bt - 2)) * 8 + tg;
  const int mw = mb * 8 + w;
  auto side_dma = [&]() {
    if (bt >= 2) {
#pragma unroll
      for (int i = 0; i < 3; ++i) {
        const int fr = min(mw + 1024 * i, 2719);
        const int fg = fr / 340, rem = fr - fg * 340, ks = rem / 5, nt = rem - ks * 5;
        const int slot = l >> 1, g_ = slot >> 3, j = slot & 7;
        const int u = (ks - 48) / 5, s_ = (ks - 48) - u * 5;
        const int id = 8 * s_ + j, e = id / 5, v = id - e * 5;
        const int d = v < 4 ? 4 * g_ + v : 16 + g_;
        const int row = ks < 48 ? 32 * ks + slot : (8 * u + e) * 20 + d;
        const char* pb_ = uptr(ks < 48 ? (const char*)wd_ : (const char*)wo_);
        dma16s(row * 1280 + (80 * fg + 16 * nt + 8 * (l & 1)) * 2, pb_, lb + L_PT + (w * 3 + i) * 1024);
      }
    }
    dma16s(ga * 3200 + min((w & 3) * 64 + l, 199) * 16, (const char*)wphi_, lb + L_WPHI + (w & 3) * 1024);
  };

  int tok = l & 15, g = l >> 4, q = (l & 15) >> 2, p = l & 3;
  auto relane = [&]() { tid = opaque(threadIdx.x); l = tid & 63; tok = l & 15; g = l >> 4; q = (l & 15) >> 2; p = l & 3; };
  const int ab = opaque(L_WR + fgi * WREG + (p >> 1) * SUB + (16 * (g >> 1) + 4 * (g & 1) + q) * 80 + 40 * (p & 1));
  const int cb = opaque(ab + 8 * tq);
  int bb[2][2][2];
#pragma unroll
  for (int h = 0; h < 2; ++h)
#pragma unroll
    for (int e = 0; e < 2; ++e)
#pragma unroll
      for (int i = 0; i < 2; ++i)
        bb[h][e][i] = opaque(L_XR + (64 * tq + 32 * (i ^ fgi) + tok) * 128 + 16 * ((4 * e + g) ^ (tok & 7)) + h * 2 * XSTG);

  f32x4 acc[5][4];
#pragma unroll
  for (int t = 0; t < 5; ++t)
#pragma unroll
    for (int i = 0; i < 4; ++i) acc[t][i] = f32x4{0.f, 0.f, 0.f, 0.f};
  f32x4 csa = f32x4{0.f, 0.f, 0.f, 0.f}, csb = f32x4{0.f, 0.f, 0.f, 0.f};
  u32x4 ONES4 = u32x4{0x3F803F80u, 0x3F803F80u, 0x3F803F80u, 0x3F803F80u};
  asm volatile("" : "+v"(ONES4));
#pragma unroll
  for (int t = 0; t < 5; ++t)
#pragma unroll
    for (int i = 0; i < 4; ++i) pin4(acc[t][i]);
  pin4(csa); pin4(csb);
  const __amdgpu_buffer_rsrc_t rsw = __builtin_amdgcn_make_buffer_rsrc((void*)ws, 0, (int)WS_BYTES, 0x00020000);
  u32x4 stv[4], fc[2];
  auto ld_shared = [&]() {
#pragma unroll
    for (int i = 0; i < 4; ++i)
      stv[i] = __builtin_amdgcn_raw_buffer_load_b128(rsw, (tg * 256 + 64 * tq + 16 * ((i + 2 * fgi) & 3) + tok) * 16, (int)OFF_ST, 16);
  };
  auto sh_bad = [&]() -> bool {
    bool bad = false;
#pragma unroll
    for (int i = 0; i < 4; ++i) bad = bad || stv[i][1] != tag || stv[i][3] != tag;
    return __any(bad);
  };

  u32x4 fa[2][5], fb[2][4];
  auto readA = [&](auto S, auto T) {
    constexpr int s = decltype(S)::value, t = decltype(T)::value;
    fa[s & 1][t] = trread2(L + ab + (s % NWS) * WSTG + 8 * t, 640);
  };
  auto readB = [&](auto S, auto I) {
    constexpr int s = decltype(S)::value, i = decltype(I)::value;
    constexpr int sl = (s / 2) % NXS, h = sl >= 2 ? 1 : 0;
    fb[s & 1][i] = *(LAS u32x4*)(L + bb[h][s & 1][i >> 1] + (sl - 2 * h) * XSTG + (i & 1) * 2048);
  };

  sfor<0, 4>([&](auto O) { issue_x(IC<1>{}, O); });
  issue_w(IC<2>{}, IC<0>{}); issue_w(IC<2>{}, IC<1>{});
  dma4(warm_addr(gw * 11 + min(l, 10), 1, x_, wqkv_, wg_, wu_), lb + L_DUMMY);
  STAMP(0, 1);
  WAIT_V(pend_at(-1));
  BAR();
  STAMP(0, 2);
  sfor<0, 4>([&](auto I) { readB(IC<0>{}, I); });
  sfor<0, 5>([&](auto T) { readA(IC<0>{}, T); });
  fc[0] = trread2(L + cb, 640);
  if (tid < 64) *(LAS u32x4*)(L + L_ZERO + tid * 16) = u32x4{0, 0, 0, 0};
  SCHED();

  sfor<0, 20>([&](auto S) {
    constexpr int s = decltype(S)::value;
    if constexpr (s + 1 < 20) {
      constexpr int pend = pend_at(s);
      asm volatile("s_waitcnt vmcnt(%0) lgkmcnt(0)" ::"i"(pend) : "memory");
      BAR();
    }
    sfor<0, 5>([&](auto T) {
      constexpr int t = decltype(T)::value;
      sfor<0, 4>([&](auto I) {
        constexpr int i = decltype(I)::value;
        mfma16a(acc[t][i], fa[s & 1][t], fb[s & 1][i]);
        if constexpr (s + 1 < 20) {
          if constexpr (t == 0) readB(IC<s + 1>{}, I);
          if constexpr (i == 1) readA(IC<s + 1>{}, T);
        }
        if constexpr (i == 3 && t < 2) issue_x(IC<s / 2 + 2>{}, IC<2 * (s & 1) + t>{});
        if constexpr (i == 3 && (t == 2 || t == 3)) issue_w(IC<s + 3>{}, IC<t - 2>{});
        if constexpr (t == 4 && i == 0 && s + 1 < 20) fc[(s + 1) & 1] = trread2(L + cb + ((s + 1) % NWS) * WSTG, 640);
        if constexpr (t == 2 && i == 1) mfma16a(csa, fc[s & 1], ONES4);
        SCHED();
      });
    });
    if ((s & 3) == tq) mfma16a(csb, fa[s & 1][4], ONES4);
    if constexpr (s == 1) {
      relane();
      pinu(xr0); pinu(xr1);
      float v[16];
#pragma unroll
      for (int j = 0; j < 4; ++j) {
        v[2 * j] = __uint_as_float(xr0[j] << 16); v[2 * j + 1] = __uint_as_float(xr0[j] & 0xffff0000u);
        v[8 + 2 * j] = __uint_as_float(xr1[j] << 16); v[9 + 2 * j] = __uint_as_float(xr1[j] & 0xffff0000u);
      }
      const bool ex = l < 16;
      float a = 0.f, c = 0.f;
#pragma unroll
      for (int j = 0; j < 8; ++j) { a += v[j]; c += v[8 + j]; }
      const float mean = wave_sum(a + (ex ? c : 0.f)) * (1.f / 640.f);
      a = 0.f; c = 0.f;
#pragma unroll
      for (int j = 0; j < 8; ++j) { const float d0 = v[j] - mean, d1 = v[8 + j] - mean; a = fmaf(d0, d0, a); c = fmaf(d1, d1, c); }
      const float var = wave_sum(a + (ex ? c : 0.f)) * (1.f / 640.f);
      gst(ws + OFF_ST + (size_t)gw * 16, u32x4{__float_as_uint(-mean), tag, __float_as_uint(var + 1e-5f), tag});
    }
    if constexpr (s == 0) { relane(); dma4(warm_addr(gw * 11 + min(l, 10), 2, x_, wqkv_, wg_, wu_), lb + L_DUMMY); }
    if constexpr (s == 1) { relane(); dma4(warm_addr2(gw * 11 + min(l, 10), wd_, wo_), lb + L_DUMMY); }
    if constexpr (s == S_SIDE) { relane(); side_dma(); ld_shared(); }
#define ST_A 4
#define ST_B 9
#define ST_C 14
    if constexpr (s == ST_A) STAMP(0, 3);
    if constexpr (s == ST_B) STAMP(0, 4);
    if constexpr (s == ST_C) STAMP(0, 5);
    SCHED();
  });
  asm volatile("s_nop 7\n\ts_nop 7" ::: "memory");
  SCHED();
  STAMP(0, 6);
  relane();
  WAIT_V(0);
  if (__builtin_expect(sh_bad(), 0)) {
    unsigned spin = 0;
    do {
      __builtin_amdgcn_s_sleep(4);
      ld_shared();
    } while (sh_bad() && ++spin < (1u << 16));
  }
  if (tok == 0) {
    *(LAS f32x4*)(L + L_CSS + (fgi * 128 + tq * 16 + 4 * g) * 4) = csa;
    *(LAS f32x4*)(L + L_CSS + (fgi * 128 + 64 + tq * 16 + 4 * g) * 4) = csb;
  }
  __syncthreads();
  float nmean[4], vare[4], rstd[4];
#pragma unroll
  for (int i = 0; i < 4; ++i) {
    nmean[i] = __uint_as_float(stv[i][0]); vare[i] = __uint_as_float(stv[i][2]);
    rstd[i] = __builtin_amdgcn_rsqf(vare[i]);
  }
#pragma unroll
  for (int t = 0; t < 5; ++t) {
    f32x4 c4 = *(LAS f32x4*)(L + L_CSS + (fgi * 128 + t * 16 + 4 * g) * 4);
    if (t == 4) {
#pragma unroll
      for (int k = 1; k < 4; ++k) c4 += *(LAS f32x4*)(L + L_CSS + (fgi * 128 + 64 + k * 16 + 4 * g) * 4);
    }
#pragma unroll
    for (int i = 0; i < 4; ++i)
#pragma unroll
      for (int c = 0; c < 4; ++c) acc[t][i][c] = fmaf(nmean[i], c4[c], acc[t][i][c]);
  }
  STAMP(0, 7);
  if (bt >= 2) {
#pragma unroll
    for (int i = 0; i < 3; ++i) {
      const u32x4 v = trread2(L + L_PT + (w * 3 + i) * 1024 + (8 * g + q) * 32 + 8 * p, 128);
      if (mw + 1024 * i < 2720) gst(ws + OFF_WT + (size_t)(mw + 1024 * i) * 1024 + l * 16, v);
    }
    const u32x4 sent = u32x4{0xFFFFFFFFu, 0xFFFFFFFFu, 0xFFFFFFFFu, 0xFFFFFFFFu};
    gst(ws + OFF_O + (size_t)mb * 20480 + tid * 16, sent);
    gst(ws + OFF_O + (size_t)mb * 20480 + 8192 + tid * 16, sent);
    if (tid < 256) gst(ws + OFF_O + (size_t)mb * 20480 + 16384 + tid * 16, sent);
    if (mb < 105 && tid < 32) gst(ws + OFF_KVT + mb * 512 + tid * 16, sent);
  }
  const int tile0 = tg * 16 + tq * 4;

  if (kind == 3) {
    const int hi = l >> 5, pp = g & 1;
#pragma unroll
    for (int i = 0; i < 2; ++i) {
      const float rs_ = hi ? rstd[i + 2] : rstd[i], ve = hi ? vare[i + 2] : vare[i];
      const float c1 = -2.0f * 0.7978845608028654f * 1.4426950408889634f;
      const float cA = c1 * rs_, cB = c1 * 0.044715f * rs_ * rs_ * rs_;
      unsigned hp[10];
#pragma unroll
      for (int t = 0; t < 5; ++t) {
#pragma unroll
        for (int c2 = 0; c2 < 2; ++c2) {
          f32x2 gg, uu;
#pragma unroll
          for (int c = 0; c < 2; ++c) {
            const float va = acc[t][i][2 * c2 + c], vb = acc[t][i + 2][2 * c2 + c];
            const auto sw = __builtin_amdgcn_permlane32_swap(__float_as_uint(va), __float_as_uint(vb), false, false);
            const unsigned s0 = sw[0], s1 = sw[1];
            gg[c] = __uint_as_float(s0); uu[c] = __uint_as_float(s1);
          }
          const f32x2 arg = __builtin_elementwise_fma(gg * gg, f32x2{cB, cB}, f32x2{cA, cA}) * gg;
          const f32x2 e = f32x2{__builtin_amdgcn_exp2f(arg[0]), __builtin_amdgcn_exp2f(arg[1])};
          const f32x2 dn = __builtin_elementwise_fma(e, f32x2{ve, ve}, f32x2{ve, ve});
          const f32x2 rr = f32x2{__builtin_amdgcn_rcpf(dn[0]), __builtin_amdgcn_rcpf(dn[1])};
          const f32x2 hv = gg * uu * rr;
          hp[2 * t + c2] = pk2(hv[0], hv[1]);
        }
      }
      const int tl = tile0 + ((i + 2 * hi + 2 * fgi) & 3);
      char* dst = ws + OFF_H + (size_t)tl * 49152 + (mbase >> 3) * 256 + tok * 16;
      if (pp == 0) {
        gst(dst, u32x4{hp[0], hp[1], hp[2], hp[3]});
        gst(dst + 256, u32x4{hp[4], hp[5], hp[6], hp[7]});
        gst(dst + 512, u32x2{hp[8], hp[9]});
      } else {
        gst(dst + 520, u32x2{hp[0], hp[1]});
        gst(dst + 768, u32x4{hp[2], hp[3], hp[4], hp[5]});
        gst(dst + 1024, u32x4{hp[6], hp[7], hp[8], hp[9]});
      }
    }
  } else {
    u32x4 bq[4][3];
    if (kind != 2) {
#pragma unroll
      for (int i = 0; i < 4; ++i) {
#pragma unroll
        for (int s = 0; s < 2; ++s)
          bq[i][s] = u32x4{pk2(acc[2 * s][i][0], acc[2 * s][i][1]), pk2(acc[2 * s][i][2], acc[2 * s][i][3]),
                           pk2(acc[2 * s + 1][i][0], acc[2 * s + 1][i][1]), pk2(acc[2 * s + 1][i][2], acc[2 * s + 1][i][3])};
        bq[i][2] = u32x4{pk2(acc[4][i][0], acc[4][i][1]), pk2(acc[4][i][2], acc[4][i][3]), 0u, 0u};
      }
    }
    if (bt == 0) {
      LAS char* xch = L + L_PK + (64 * tq + 32) * PK_PITCH + l * 16;
      if (kind == 1) {
#pragma unroll
        for (int j = 0; j < 6; ++j) *(LAS u32x4*)(xch + j * 1024) = bq[2 + j / 3][j % 3];
      } else {
#pragma unroll
        for (int i = 0; i < 4; ++i) {
          const int trow = 64 * tq + 16 * ((i + 2) & 3) + tok;
#pragma unroll
          for (int t = 0; t < 5; ++t)
            *(LAS u32x2*)(L + L_V + trow * V_PITCH + 40 * g + 8 * t) =
                u32x2{pk2(acc[t][i][0] * rstd[i], acc[t][i][1] * rstd[i]), pk2(acc[t][i][2] * rstd[i], acc[t][i][3] * rstd[i])};
          if (g == 0) *(LAS u32x2*)(L + L_V + trow * V_PITCH + 160) = u32x2{0x00003F80u, 0u};
        }
      }
      __syncthreads();
      if (kind == 2) {
#pragma unroll
        for (int j = 0; j < 6; ++j) bq[j / 3][j % 3] = *(LAS u32x4*)(xch + j * 1024);
      }
    }
    const int wb = (p == g) ? (L_WPHI + g * 800 + q * 40) : L_ZERO;
    auto phi2 = [&](auto I0) {
      constexpr int i0 = decltype(I0)::value;
      f32x4 pq[5][2];
#pragma unroll
      for (int tt = 0; tt < 5; ++tt) {
        const u32x4 a0 = trread2(L + wb + 8 * tt, 160), a1 = trread2(L + wb + 8 * tt + 320, 160);
        const u32x2 a2l = trread(L + wb + 8 * tt + 640);
        const u32x4 a2 = u32x4{a2l[0], a2l[1], 0u, 0u};
#pragma unroll
        for (int i = 0; i < 2; ++i) {
          f32x4 o = f32x4{0.f, 0.f, 0.f, 0.f};
          o = mfma16(a0, bq[i0 + i][0], o);
          o = mfma16(a1, bq[i0 + i][1], o);
          o = mfma16(a2, bq[i0 + i][2], o);
          pq[tt][i] = o;
        }
      }
#pragma unroll
      for (int i = 0; i < 2; ++i) {
        const float c1 = rstd[i0 + i] * 1.4426950408889634f;
        float mx = pq[0][i][0], mn = pq[0][i][0];
#pragma unroll
        for (int f = 1; f < 20; ++f) { mx = fmaxf(mx, pq[f >> 2][i][f & 3]); mn = fminf(mn, pq[f >> 2][i][f & 3]); }
        const float bx = -mx * c1, bn = mn * c1;
        f32x2 ep[10], en[10], sp2 = f32x2{0.f, 0.f}, sn2 = f32x2{0.f, 0.f};
#pragma unroll
        for (int f = 0; f < 10; ++f) {
          const f32x2 pv = f32x2{pq[f >> 1][i][2 * (f & 1)], pq[f >> 1][i][2 * (f & 1) + 1]};
          const f32x2 ap = __builtin_elementwise_fma(pv, f32x2{c1, c1}, f32x2{bx, bx});
          const f32x2 an = __builtin_elementwise_fma(pv, f32x2{-c1, -c1}, f32x2{bn, bn});
          ep[f] = f32x2{__builtin_amdgcn_exp2f(ap[0]), __builtin_amdgcn_exp2f(ap[1])};
          en[f] = f32x2{__builtin_amdgcn_exp2f(an[0]), __builtin_amdgcn_exp2f(an[1])};
          sp2 += ep[f]; sn2 += en[f];
        }
        const float rp = __builtin_amdgcn_rcpf(sp2[0] + sp2[1]), rn = __builtin_amdgcn_rcpf(sn2[0] + sn2[1]);
        unsigned o[20];
#pragma unroll
        for (int f = 0; f < 10; ++f) {
          const f32x2 a = ep[f] * f32x2{rp, rp}, c = en[f] * f32x2{rn, rn};
          o[f] = pk2(a[0], a[1]);
          o[10 + f] = pk2(c[0], c[1]);
        }
        if (kind == 0) {
          char* dst = ws + OFF_PQ + (size_t)((tile0 + i0 + i) * 8 + ga) * 5120 + tok * 16;
#pragma unroll
          for (int ci = 0; ci < 4; ++ci) gst(dst + g * 1024 + ci * 256, u32x4{o[4 * ci], o[4 * ci + 1], o[4 * ci + 2], o[4 * ci + 3]});
          gst(dst + 4096 + g * 256, u32x4{o[16], o[17], o[18], o[19]});
        } else {
          const int trow = 64 * tq + 32 * fgi + 16 * i + tok;
#pragma unroll
          for (int ci = 0; ci < 5; ++ci)
            *(LAS u32x4*)(L + L_PK + trow * PK_PITCH + g * 80 + ci * 16) = u32x4{o[4 * ci], o[4 * ci + 1], o[4 * ci + 2], o[4 * ci + 3]};
        }
      }
    };
    phi2(IC<0>{});
    if (kind == 0) phi2(IC<2>{});
  }
  if (bt == 0) {
    __syncthreads();
    const int e = w >> 1, bh = w & 1;
    const int rowoff = 8 * g + q;
    LAS char* pa = L + L_PK + rowoff * PK_PITCH + e * 80 + 8 * p;
    LAS char* pa2 = L + L_PK + rowoff * PK_PITCH + e * 80 + 64 + 8 * (p & 1);
    LAS char* pb = L + L_V + rowoff * V_PITCH + (bh == 0 ? e * 40 + 8 * p : (p == 0 ? e * 40 + 32 : 160));
    f32x4 d[3];
#pragma unroll
    for (int k = 0; k < 3; ++k) d[k] = f32x4{0.f, 0.f, 0.f, 0.f};
#pragma unroll
    for (int ks = 0; ks < 8; ++ks) {
      const u32x4 bb = trread2(pb + ks * 32 * V_PITCH, 4 * V_PITCH);
      d[0] = mfma16(trread2(pa + ks * 32 * PK_PITCH, 4 * PK_PITCH), bb, d[0]);
      d[1] = mfma16(trread2(pa + ks * 32 * PK_PITCH + 32, 4 * PK_PITCH), bb, d[1]);
      d[2] = mfma16(trread2(pa2 + ks * 32 * PK_PITCH, 4 * PK_PITCH), bb, d[2]);
    }
    const int dcol = 16 * bh + tok;
    if (dcol <= 20) {
      float* dst = (float*)(ws + OFF_KVP) + ((size_t)(tg * 32 + 4 * ga + e) * 21 + dcol) * 40 + 4 * g;
      *(f32x4*)(dst) = d[0];
      *(f32x4*)(dst + 16) = d[1];
      if (g < 2) *(f32x4*)(dst + 32) = d[2];
    }
  }
  asm volatile("" ::"v"(cpf));
  STAMP(0, 8);
  WAIT_V(0);
  STAMP_FLUSH(0);
}

constexpr int K2_LDS = 163840;
__global__ __launch_bounds__(512, 2) void k2_out(const __hip_bfloat16* __restrict__ x_, char* __restrict__ ws,
                                                 __hip_bfloat16* __restrict__ out_) {
  extern __shared__ __attribute__((aligned(16))) char smem[];
  LAS char* L = (LAS char*)smem;
  const int tid = threadIdx.x, l = tid & 63;
  const int w = __builtin_amdgcn_readfirstlane(tid >> 6);
  const int b = blockIdx.x, xcd = b & 7, idx = b >> 3;
  STAMP_DECL;
  STAMP(1, 0);
  const __amdgpu_buffer_rsrc_t rs = __builtin_amdgcn_make_buffer_rsrc((void*)ws, 0, (int)WS_BYTES, 0x00020000);
  const bool red = (w == 7 && b < 210);
  f32x2 pv[NP];
  if (w == 7) __builtin_amdgcn_s_setprio(3);
  if (red) {
    const GAS f32x2* pp = (const GAS f32x2*)(ws + OFF_KVP) + b * 64 + l;
#pragma unroll
    for (int k = 0; k < NP; ++k) pv[k] = pp[k * 13440];
  }
  SCHED();
  BAR();
  const unsigned cpf = code_prefetch<K2_PF>(idx, tid);
  const int tg = xcd * 4 + (idx & 3);
  const int fg = idx >> 2;
  const int vo = l * 16;
  const int so_h = (int)OFF_H + tg * 4 * 49152 + 6 * w * 1024;
  const int so_a = (int)OFF_WT + fg * 68 * 5120 + 6 * w * 5120;
  const int ok0 = w < 4 ? 2 * w : 3 * w - 4;
  const int so_o = (int)OFF_WT + fg * 68 * 5120 + (48 + ok0) * 5120;
  const int so_f = (int)OFF_O + (tg * 80 + ok0) * 1024;
  auto ldg = [&](int so) -> u32x4 { return __builtin_amdgcn_raw_buffer_load_b128(rs, vo, so, 0); };
  f32x4 acc[5][4];
#pragma unroll
  for (int nt = 0; nt < 5; ++nt)
#pragma unroll
    for (int i = 0; i < 4; ++i) { acc[nt][i] = f32x4{0.f, 0.f, 0.f, 0.f}; pin4(acc[nt][i]); }

  u32x4 fa[6][5], fb[6][4], ca[3][5], ob[3][4];
  auto ld = [&](auto K) {
    constexpr int k = decltype(K)::value;
#pragma unroll
    for (int i = 0; i < 4; ++i) fb[k][i] = ldg(so_h + i * 49152 + k * 1024);
#pragma unroll
    for (int nt = 0; nt < 5; ++nt) fa[k][nt] = ldg(so_a + k * 5120 + nt * 1024);
    SCHED();
  };
  auto mm = [&](auto K) {
    constexpr int k = decltype(K)::value;
#pragma unroll
    for (int nt = 0; nt < 5; ++nt)
#pragma unroll
      for (int i = 0; i < 4; ++i) mfma16a(acc[nt][i], fa[k][nt], fb[k][i]);
    SCHED();
  };
  auto ldo = [&](int s) {
#pragma unroll
    for (int nt = 0; nt < 5; ++nt) ca[s][nt] = ldg(so_o + s * 5120 + nt * 1024);
    SCHED();
  };
  auto ldf = [&](int s) {
#pragma unroll
    for (int i = 0; i < 4; ++i) ob[s][i] = __builtin_amdgcn_raw_buffer_load_b128(rs, vo, so_f + i * 20480 + s * 1024, 16);
  };
  auto fbad = [&](int s) -> bool {
    unsigned mxw = 0u;
#pragma unroll
    for (int i = 0; i < 4; ++i)
#pragma unroll
      for (int j = 0; j < 4; ++j) mxw = max(mxw, ob[s][i][j]);
    return __any(mxw == 0xFFFFFFFFu);
  };
  auto ostep = [&](int s) {
    if (__builtin_expect(fbad(s), 0)) {
      unsigned spin = 0;
      do {
        __builtin_amdgcn_s_sleep(4);
        ldf(s);
      } while (fbad(s) && ++spin < (1u << 14));
    }
#pragma unroll
    for (int nt = 0; nt < 5; ++nt)
#pragma unroll
      for (int i = 0; i < 4; ++i) mfma16a(acc[nt][i], ca[s][nt], ob[s][i]);
    SCHED();
  };

  if (red) {
    f32x2 s = pv[0];
#pragma unroll
    for (int k = 1; k < NP; ++k) s += pv[k];
    __builtin_amdgcn_raw_buffer_store_b32(pk2(s[0], s[1]), rs, (int)OFF_KVT + b * 256 + l * 4, 0, 16);
  }
  __builtin_amdgcn_s_setprio(3);
  SCHED();
  STAMP(1, 1);
  ld(IC<0>{}); ld(IC<1>{}); ld(IC<2>{});
  __builtin_amdgcn_s_setprio(2);
  mm(IC<0>{}); ld(IC<3>{});
  asm volatile("" ::"v"(cpf));
  mm(IC<1>{});
  if (w < 4) {
    const int r = l & 15, g = l >> 4;
    const int row1 = (r & 3) == 0 ? 16 + (r >> 2) : 20;
    const int so_k = (int)OFF_KVT + 4 * fg * 1680;
    const int ko0 = r * 80 + g * 16, ko1 = row1 * 80 + g * 16, kt0 = r * 80 + g * 1680 + 64, kt1 = row1 * 80 + g * 1680 + 64;
    u32x4 pf[5], ka[4][2], kt[2];
#pragma unroll
    for (int f = 0; f < 5; ++f) pf[f] = ldg((int)OFF_PQ + ((tg * 4 + w) * 8 + fg) * 5120 + f * 1024);
    for (unsigned spin = 0; spin < (1u << 14); ++spin) {
#pragma unroll
      for (int hd = 0; hd < 4; ++hd) {
        ka[hd][0] = __builtin_amdgcn_raw_buffer_load_b128(rs, ko0, so_k + hd * 1680, 16);
        ka[hd][1] = __builtin_amdgcn_raw_buffer_load_b128(rs, ko1, so_k + hd * 1680, 16);
      }
      kt[0] = __builtin_amdgcn_raw_buffer_load_b128(rs, kt0, so_k, 16);
      kt[1] = __builtin_amdgcn_raw_buffer_load_b128(rs, kt1, so_k, 16);
      unsigned mxw = 0u;
#pragma unroll
      for (int k = 0; k < 2; ++k)
#pragma unroll
        for (int j = 0; j < 4; ++j) {
          mxw = max(mxw, kt[k][j]);
#pragma unroll
          for (int hd = 0; hd < 4; ++hd) mxw = max(mxw, ka[hd][k][j]);
        }
      if (!__any(mxw == 0xFFFFFFFFu)) break;
      __builtin_amdgcn_s_sleep(4);
    }
    float ov[20];
#pragma unroll
    for (int hd = 0; hd < 4; ++hd) {
      const u32x4 pt = (g == hd) ? pf[4] : u32x4{0u, 0u, 0u, 0u};
      f32x4 o0 = f32x4{0.f, 0.f, 0.f, 0.f}, o1 = f32x4{0.f, 0.f, 0.f, 0.f};
      o0 = mfma16(ka[hd][0], pf[hd], o0);
      o1 = mfma16(ka[hd][1], pf[hd], o1);
      o0 = mfma16(kt[0], pt, o0);
      o1 = mfma16(kt[1], pt, o1);
      const float inv = __builtin_amdgcn_rcpf(o1[1] + 1e-6f);
#pragma unroll
      for (int v = 0; v < 4; ++v) ov[5 * hd + v] = o0[v] * inv;
      ov[5 * hd + 4] = o1[0] * inv;
    }
    unsigned op[10];
#pragma unroll
    for (int j = 0; j < 10; ++j) op[j] = pk2(ov[2 * j], ov[2 * j + 1]);
    const int so_d = (int)OFF_O + ((tg * 4 + w) * 20 + (fg >> 1) * 5) * 1024;
    if ((fg & 1) == 0) {
      __builtin_amdgcn_raw_buffer_store_b128(u32x4{op[0], op[1], op[2], op[3]}, rs, vo, so_d, 16);
      __builtin_amdgcn_raw_buffer_store_b128(u32x4{op[4], op[5], op[6], op[7]}, rs, vo, so_d + 1024, 16);
      __builtin_amdgcn_raw_buffer_store_b64(u32x2{op[8], op[9]}, rs, vo, so_d + 2048, 16);
    } else {
      __builtin_amdgcn_raw_buffer_store_b64(u32x2{op[0], op[1]}, rs, vo + 8, so_d + 2048, 16);
      __builtin_amdgcn_raw_buffer_store_b128(u32x4{op[2], op[3], op[4], op[5]}, rs, vo, so_d + 3072, 16);
      __builtin_amdgcn_raw_buffer_store_b128(u32x4{op[6], op[7], op[8], op[9]}, rs, vo, so_d + 4096, 16);
    }
  }
  SCHED();
  STAMP(1, 2);
  ld(IC<4>{});
  __builtin_amdgcn_s_setprio(1);
  mm(IC<2>{}); ld(IC<5>{});
  mm(IC<3>{}); ldo(0); ldo(1);
  __builtin_amdgcn_s_setprio(0);
  mm(IC<4>{}); ldf(0); ldf(1); SCHED();
  if (w >= 4) { ldo(2); ldf(2); }
  SCHED();
  u32x2 xv[3];
#pragma unroll
  for (int k = 0; k < 3; ++k) {
    const int t = min(w + 8 * k, 19), nt = t >> 2, i = t & 3;
    xv[k] = *(const u32x2*)((const char*)x_ + ((size_t)(tg * 64 + i * 16 + (l & 15)) * 640 + fg * 80 + nt * 16 + 4 * (l >> 4)) * 2);
  }
  SCHED();
  mm(IC<5>{});
  STAMP(1, 3);
  ostep(0);
  ostep(1);
  if (w >= 4) ostep(2);
  SCHED();
  asm volatile("s_nop 7\n\ts_nop 7" ::: "memory");
  SCHED();
  STAMP(1, 4);
#pragma unroll
  for (int nt = 0; nt < 5; ++nt)
#pragma unroll
    for (int i = 0; i < 4; ++i) *(LAS f32x4*)(L + ((w * 20 + nt * 4 + i) * 64 + l) * 16) = acc[nt][i];
  __syncthreads();
  STAMP(1, 5);
#pragma unroll
  for (int k = 0; k < 3; ++k) {
    const int t = w + 8 * k;
    if (t < 20) {
      f32x4 s = *(LAS f32x4*)(L + (t * 64 + l) * 16);
#pragma unroll
      for (int ww = 1; ww < 8; ++ww) s += *(LAS f32x4*)(L + ((ww * 20 + t) * 64 + l) * 16);
      const int nt = t >> 2, i = t & 3;
      const size_t off = ((size_t)(tg * 64 + i * 16 + (l & 15)) * 640 + fg * 80 + nt * 16 + 4 * (l >> 4)) * 2;
      const float x0 = __builtin_bit_cast(float, xv[k][0] << 16), x1 = __builtin_bit_cast(float, xv[k][0] & 0xffff0000u);
      const float x2 = __builtin_bit_cast(float, xv[k][1] << 16), x3 = __builtin_bit_cast(float, xv[k][1] & 0xffff0000u);
      *(u32x2*)((char*)out_ + off) = u32x2{pk2(s[0] + x0, s[1] + x1), pk2(s[2] + x2, s[3] + x3)};
    }
  }
  STAMP(1, 6);
  STAMP_FLUSH(1);
}

extern "C" void kernel_launch(void* const* d_in, const int* in_sizes, int n_in, void* d_out, int out_size, void* d_ws,
                              size_t ws_size, hipStream_t stream) {
  const __hip_bfloat16* x = (const __hip_bfloat16*)d_in[0];
  const __hip_bfloat16* wqkv = (const __hip_bfloat16*)d_in[1];
  const __hip_bfloat16* wphi = (const __hip_bfloat16*)d_in[2];
  const __hip_bfloat16* wo = (const __hip_bfloat16*)d_in[3];
  const __hip_bfloat16* wg = (const __hip_bfloat16*)d_in[5];
  const __hip_bfloat16* wu = (const __hip_bfloat16*)d_in[6];
  const __hip_bfloat16* wd = (const __hip_bfloat16*)d_in[7];
  (void)hipFuncSetAttribute((const void*)k1_proj, hipFuncAttributeMaxDynamicSharedMemorySize, K1_LDS);
  (void)hipFuncSetAttribute((const void*)k2_out, hipFuncAttributeMaxDynamicSharedMemorySize, K2_LDS);
  k1_proj<<<256, 512, K1_LDS, stream>>>(x, wqkv, wphi, wo, wg, wu, wd, (char*)d_ws);
  k2_out<<<256, 512, K2_LDS, stream>>>(x, (char*)d_ws, (__hip_bfloat16*)d_out);
}
```

```cpp
#include <hip/hip_runtime.h>
#include <hip/hip_bf16.h>
#include <stdint.h>

#define GAS __attribute__((address_space(1)))
#define LAS __attribute__((address_space(3)))
#define DI __device__ __forceinline__
typedef __bf16 bf16x8 __attribute__((ext_vector_type(8)));
typedef __bf16 bf16x2 __attribute__((ext_vector_type(2)));
typedef short s16x4 __attribute__((ext_vector_type(4)));
typedef float f32x4 __attribute__((ext_vector_type(4)));
typedef float f32x2 __attribute__((ext_vector_type(2)));
typedef unsigned u32x4 __attribute__((ext_vector_type(4)));
typedef unsigned u32x2 __attribute__((ext_vector_type(2)));

extern "C" __device__ unsigned long long llvm_dispatch_id() __asm("llvm.amdgcn.dispatch.id");
template <int N> struct IC { static constexpr int value = N; };
template <int B, int E, class F> DI void sfor(F&& f) {
  if constexpr (B < E) { f(IC<B>{}); sfor<B + 1, E>(f); }
}

constexpr unsigned OFF_H = 0;
constexpr unsigned OFF_PQ = 6291456;
constexpr unsigned OFF_WT = 11534336;
constexpr unsigned OFF_KVP = 14483456;
constexpr unsigned OFF_KVT = 15343616;
constexpr unsigned OFF_ST = 15397376;
constexpr unsigned OFF_O = OFF_ST + 32768;
constexpr unsigned WS_BYTES = OFF_O + 2621440;
constexpr int NP = 8;

constexpr int SUB = 2688;
constexpr int WREG = SUB + 2560;
constexpr int WSTG = 2 * WREG;
constexpr int XSTG = 32768;
constexpr int NXS = 3, NWS = 4;
constexpr int L_WPHI = 0;
constexpr int L_ZERO = L_WPHI + 4096;
constexpr int L_ST = L_ZERO + 1024;
constexpr int L_CSS = L_ST + 2048;
constexpr int L_DUMMY = L_CSS + 2048;
constexpr int L_RING = L_CSS + 2560;
constexpr int L_XR = L_RING, L_WR = L_XR + NXS * XSTG;
constexpr int L_PT = L_XR + XSTG;
constexpr int K1_LDS = L_WR + NWS * WSTG;
constexpr int PK_PITCH = 336, V_PITCH = 176;
constexpr int L_PK = L_RING, L_V = L_PK + 256 * PK_PITCH;

DI unsigned pk2(float a, float b) {
  bf16x2 v; v[0] = (__bf16)a; v[1] = (__bf16)b;
  return __builtin_bit_cast(unsigned, v);
}
DI u32x2 trread(LAS char* p) {
  s16x4 v = __builtin_amdgcn_ds_read_tr16_b64_v4i16((LAS s16x4*)p);
  return __builtin_bit_cast(u32x2, v);
}
DI u32x4 trread2(LAS char* p, int hi) {
  u32x2 a = trread(p), b = trread(p + hi);
  return u32x4{a[0], a[1], b[0], b[1]};
}
DI f32x4 mfma16(u32x4 a, u32x4 b, f32x4 c) {
  return __builtin_amdgcn_mfma_f32_16x16x32_bf16(__builtin_bit_cast(bf16x8, a), __builtin_bit_cast(bf16x8, b), c, 0, 0, 0);
}
DI void mfma16a(f32x4& c, const u32x4& a, const u32x4& b) {
  asm volatile("v_mfma_f32_16x16x32_bf16 %0, %1, %2, %0" : "+v"(c) : "v"(a), "v"(b));
}
DI void gload16a(u32x4& dst, unsigned voff, const char* sbase) {
  asm volatile("global_load_dwordx4 %0, %1, %2" : "=v"(dst) : "v"(voff), "s"(sbase));
}
template <int CTRL, int RM> DI float dpp_add(float v) {
  return v + __int_as_float(__builtin_amdgcn_update_dpp(0, __float_as_int(v), CTRL, RM, 0xf, false));
}
DI float wave_sum(float v) {
  v = dpp_add<0x111, 0xf>(v);
  v = dpp_add<0x112, 0xf>(v);
  v = dpp_add<0x114, 0xf>(v);
  v = dpp_add<0x118, 0xf>(v);
  v = dpp_add<0x142, 0xa>(v);
  v = dpp_add<0x143, 0xc>(v);
  return __int_as_float(__builtin_amdgcn_readlane(__float_as_int(v), 63));
}
DI void dma4(const void* g, int ldsaddr) {
  asm volatile("s_mov_b32 m0, %1\n\ts_nop 0\n\tglobal_load_lds_dword %0, off" ::"v"(g), "s"(ldsaddr) : "memory");
}
template <int OFF = 0> DI void dma16s(unsigned voff, const char* sbase, int ldsaddr) {
  asm volatile("s_mov_b32 m0, %2\n\ts_nop 0\n\tglobal_load_lds_dwordx4 %0, %1 offset:%3" ::"v"(voff), "s"(sbase), "s"(ldsaddr - OFF), "i"(OFF) : "memory");
}
DI void dma16s_q(unsigned voff, const char* sbase, int ldsaddr) {
  asm volatile("s_mov_b32 m0, %2\n\ts_mov_b64 exec, 0xffff\n\tglobal_load_lds_dwordx4 %0, %1\n\ts_mov_b64 exec, -1" ::"v"(voff), "s"(sbase),
               "s"(ldsaddr)
               : "memory");
}
DI int opaque(int v) { asm volatile("" : "+v"(v)); return v; }
DI void pin4(f32x4& v) { asm volatile("" : "+v"(v)); }
DI void pinu(u32x4& v) { asm volatile("" : "+v"(v)); }
DI void gst(char* p, u32x4 v) { asm volatile("global_store_dwordx4 %0, %1, off sc1\n\ts_nop 1" ::"v"(p), "v"(v) : "memory"); }
DI void gst(char* p, u32x2 v) { asm volatile("global_store_dwordx2 %0, %1, off sc1\n\ts_nop 1" ::"v"(p), "v"(v) : "memory"); }
#define WAIT_V(n) asm volatile("s_waitcnt vmcnt(%0)" ::"i"(n) : "memory")
#define STAMP_DECL
#define STAMP(k, i)
#define STAMP_FLUSH(k)
template <int NL> DI unsigned code_prefetch(int idx, int tid) {
  unsigned long long pc;
  asm volatile("s_getpc_b64 %0" : "=s"(pc));
  unsigned v = 0;
  if (tid < NL) v = *(const GAS unsigned*)(pc + (unsigned long long)(idx * NL + tid) * 64);
  return v;
}
DI const char* uptr(const char* p) {
  const unsigned long long v = (unsigned long long)p;
  const unsigned lo = __builtin_amdgcn_readfirstlane((unsigned)v), hi = __builtin_amdgcn_readfirstlane((unsigned)(v >> 32));
  return (const char*)(((unsigned long long)hi << 32) | lo);
}
#define BAR() __builtin_amdgcn_s_barrier()
#define SCHED() __builtin_amdgcn_sched_barrier(0)
#define K1_PF 15
#define K2_PF 3

constexpr int NB = 1;
constexpr int S_SIDE = 16;
constexpr int xtra(int s) { return (s < 2 ? NB : 0) + (s == 1 ? 1 : 0) + (s == S_SIDE ? 1 + 4 : 0); }
constexpr int pend_at(int s) {
  int n = 0;
  auto op = [&](int cnt, int need) { if (need <= s + 1) n = 0; else n += cnt; };
  op(4, 0); op(2, 0); op(2, 1); op(NB + 2, 99); op(4, 2); op(2, 2); op(NB, 99);
  for (int st = 0; st < s; ++st) {
    if (st / 2 + 2 < 10) op(2, 2 * (st / 2 + 2));
    if (st + 3 < 20) op(2, st + 3);
    op(xtra(st), 99);
  }
  return n;
}
DI const char* warm_addr(int n, int j, const void* px, const void* pq, const void* pg, const void* pu) {
  n = min(n, 21119);
  const int xr = n / 3, xl = n - 3 * xr;
  const int m = max(n - 6144, 0), r = m / 78, c = m - 78 * r;
  const unsigned k = 64 + 192 * j + r;
  unsigned long long a = (unsigned long long)pq + k * 3840u + c * 128u;
  a = c >= 30 ? (unsigned long long)pg + k * 3072u + (c - 30) * 128u : a;
  a = c >= 54 ? (unsigned long long)pu + k * 3072u + (c - 54) * 128u : a;
  a = n < 6144 ? (unsigned long long)px + xr * 1280u + (1 + 3 * j + xl) * 128u : a;
  return (const char*)a;
}
DI const char* warm_addr2(int n, const void* pd, const void* po) {
  n = min(n, 21759);
  return (const char*)((n < 15360 ? (unsigned long long)pd : (unsigned long long)po - 15360ull * 128) + (unsigned long long)n * 128);
}

__global__ __launch_bounds__(512, 2) void k1_proj(const __hip_bfloat16* __restrict__ x_, const __hip_bfloat16* __restrict__ wqkv_,
                                                  const __hip_bfloat16* __restrict__ wphi_, const __hip_bfloat16* __restrict__ wo_,
                                                  const __hip_bfloat16* __restrict__ wg_, const __hip_bfloat16* __restrict__ wu_,
                                                  const __hip_bfloat16* __restrict__ wd_, char* __restrict__ ws) {
  extern __shared__ __attribute__((aligned(16))) char smem[];
  LAS char* L = (LAS char*)smem;
  const int lb = (int)(size_t)L;
  STAMP_DECL;
  STAMP(0, 0);
  int tid = threadIdx.x, l = tid & 63;
  const int w = __builtin_amdgcn_readfirstlane(tid >> 6);
  const int tq = w & 3, fgi = w >> 2;
  const int b = blockIdx.x, xcd = b & 7, idx = b >> 3;
  const unsigned cpf = code_prefetch<K1_PF>(idx, tid);
  const int tg = (xcd & 1) * 4 + (idx & 3);
  const int cg = (xcd >> 1) * 8 + (idx >> 2);
  const int ga = cg >> 2, bt = cg & 3;
  const int kind = bt == 0 ? 1 + fgi : ((bt == 1 && fgi == 0) ? 0 : 3);
  const int mj = bt == 1 ? ga : 8 + 4 * ga + 2 * (bt - 2) + fgi;
  const int mbase = min(40 * mj, 1496);

  const unsigned xo0 = (32 * w + (l >> 3)) * 1280 + 16 * ((l & 7) ^ ((l >> 3) & 7));
  const char* xs = uptr((const char*)x_ + (size_t)tg * 256 * 1280);
  const int wsub = tq >> 1;
  const int rs = kind == 3 ? 3072 : 3840;
  unsigned wo[2];
#pragma unroll
  for (int i = 0; i < 2; ++i) {
    const int c = min(64 * i + l, 79), pl = c / 5, ch = c - pl * 5;
    const int pos = 16 * (tq & 1) + pl;
    const int krow = (pos & 19) | ((pos & 4) << 1) | ((pos & 8) >> 1);
    wo[i] = krow * rs + 16 * ch;
  }
  const char* wsrc = uptr(kind == 3 ? (const char*)(wsub ? wu_ : wg_) + mbase * 2
                                    : (const char*)wqkv_ + (640 * kind + 80 * ga + 40 * wsub) * 2);
  const int winc = 32 * rs;
  const int xdst = lb + L_XR + w * 4096;
  const int wdst = lb + L_WR + fgi * WREG + wsub * SUB + (tq & 1) * 1280;
  auto issue_x = [&](auto J, auto O) {
    constexpr int j = decltype(J)::value, o = decltype(O)::value;
    if constexpr (j < 10) dma16s<j * 128>(xo0, xs + o * 10240, xdst + (j % NXS) * XSTG + o * 1024);
  };
  auto issue_w = [&](auto S, auto O) {
    constexpr int s = decltype(S)::value, o = decltype(O)::value;
    if constexpr (s < 20) {
      if constexpr (o == 0) dma16s(wo[0], wsrc, wdst + (s % NWS) * WSTG);
      if constexpr (o == 1) { dma16s_q(wo[1], wsrc, wdst + (s % NWS) * WSTG + 1024); wsrc += winc; }
    }
  };
  sfor<0, 4>([&](auto O) { issue_x(IC<0>{}, O); });
  issue_w(IC<0>{}, IC<0>{}); issue_w(IC<0>{}, IC<1>{});
  BAR();
  issue_w(IC<1>{}, IC<0>{}); issue_w(IC<1>{}, IC<1>{});
  const int gw = b * 8 + w;
  dma4(warm_addr(gw * 11 + min(l, 10), 0, x_, wqkv_, wg_, wu_), lb + L_DUMMY);
  const unsigned tag = ((unsigned)llvm_dispatch_id() + 1u) * 0x9E3779B1u ^ 0x5BD1E995u;
  u32x4 xr0, xr1;
  {
    const char* xrow = uptr((const char*)x_ + (size_t)gw * 1280);
    gload16a(xr0, l * 16, xrow);
    gload16a(xr1, 1024 + (l & 15) * 16, xrow);
  }
  const int mb = (ga * 2 + (bt - 2)) * 8 + tg;
  const int mw = mb * 8 + w;
  auto side_dma = [&]() {
    if (bt >= 2) {
#pragma unroll
      for (int i = 0; i < 3; ++i) {
        const int fr = min(mw + 1024 * i, 2719);
        const int fg = fr / 340, rem = fr - fg * 340, ks = rem / 5, nt = rem - ks * 5;
        const int slot = l >> 1, g_ = slot >> 3, j = slot & 7;
        const int u = (ks - 48) / 5, s_ = (ks - 48) - u * 5;
        const int id = 8 * s_ + j, e = id / 5, v = id - e * 5;
        const int d = v < 4 ? 4 * g_ + v : 16 + g_;
        const int row = ks < 48 ? 32 * ks + slot : (8 * u + e) * 20 + d;
        const char* pb_ = uptr(ks < 48 ? (const char*)wd_ : (const char*)wo_);
        dma16s(row * 1280 + (80 * fg + 16 * nt + 8 * (l & 1)) * 2, pb_, lb + L_PT + (w * 3 + i) * 1024);
      }
    }
    dma16s(ga * 3200 + min((w & 3) * 64 + l, 199) * 16, (const char*)wphi_, lb + L_WPHI + (w & 3) * 1024);
  };

  int tok = l & 15, g = l >> 4, q = (l & 15) >> 2, p = l & 3;
  auto relane = [&]() { tid = opaque(threadIdx.x); l = tid & 63; tok = l & 15; g = l >> 4; q = (l & 15) >> 2; p = l & 3; };
  const int ab = opaque(L_WR + fgi * WREG + (p >> 1) * SUB + (16 * (g >> 1) + 4 * (g & 1) + q) * 80 + 40 * (p & 1));
  const int cb = opaque(ab + 8 * tq);
  int bb[2][2][2];
#pragma unroll
  for (int h = 0; h < 2; ++h)
#pragma unroll
    for (int e = 0; e < 2; ++e)
#pragma unroll
      for (int i = 0; i < 2; ++i)
        bb[h][e][i] = opaque(L_XR + (64 * tq + 32 * (i ^ fgi) + tok) * 128 + 16 * ((4 * e + g) ^ (tok & 7)) + h * 2 * XSTG);

  f32x4 acc[5][4];
#pragma unroll
  for (int t = 0; t < 5; ++t)
#pragma unroll
    for (int i = 0; i < 4; ++i) acc[t][i] = f32x4{0.f, 0.f, 0.f, 0.f};
  f32x4 csa = f32x4{0.f, 0.f, 0.f, 0.f}, csb = f32x4{0.f, 0.f, 0.f, 0.f};
  u32x4 ONES4 = u32x4{0x3F803F80u, 0x3F803F80u, 0x3F803F80u, 0x3F803F80u};
  asm volatile("" : "+v"(ONES4));
#pragma unroll
  for (int t = 0; t < 5; ++t)
#pragma unroll
    for (int i = 0; i < 4; ++i) pin4(acc[t][i]);
  pin4(csa); pin4(csb);
  const __amdgpu_buffer_rsrc_t rsw = __builtin_amdgcn_make_buffer_rsrc((void*)ws, 0, (int)WS_BYTES, 0x00020000);
  u32x4 stv[4], fc[2];
  auto ld_shared = [&]() {
#pragma unroll
    for (int i = 0; i < 4; ++i)
      stv[i] = __builtin_amdgcn_raw_buffer_load_b128(rsw, (tg * 256 + 64 * tq + 16 * ((i + 2 * fgi) & 3) + tok) * 16, (int)OFF_ST, 16);
  };
  auto sh_bad = [&]() -> bool {
    bool bad = false;
#pragma unroll
    for (int i = 0; i < 4; ++i) bad = bad || stv[i][1] != tag || stv[i][3] != tag;
    return __any(bad);
  };

  u32x4 fa[2][5], fb[2][4];
  auto readA = [&](auto S, auto T) {
    constexpr int s = decltype(S)::value, t = decltype(T)::value;
    fa[s & 1][t] = trread2(L + ab + (s % NWS) * WSTG + 8 * t, 640);
  };
  auto readB = [&](auto S, auto I) {
    constexpr int s = decltype(S)::value, i = decltype(I)::value;
    constexpr int sl = (s / 2) % NXS, h = sl >= 2 ? 1 : 0;
    fb[s & 1][i] = *(LAS u32x4*)(L + bb[h][s & 1][i >> 1] + (sl - 2 * h) * XSTG + (i & 1) * 2048);
  };

  sfor<0, 4>([&](auto O) { issue_x(IC<1>{}, O); });
  issue_w(IC<2>{}, IC<0>{}); issue_w(IC<2>{}, IC<1>{});
  dma4(warm_addr(gw * 11 + min(l, 10), 1, x_, wqkv_, wg_, wu_), lb + L_DUMMY);
  STAMP(0, 1);
  WAIT_V(pend_at(-1));
  BAR();
  STAMP(0, 2);
  sfor<0, 4>([&](auto I) { readB(IC<0>{}, I); });
  sfor<0, 5>([&](auto T) { readA(IC<0>{}, T); });
  fc[0] = trread2(L + cb, 640);
  if (tid < 64) *(LAS u32x4*)(L + L_ZERO + tid * 16) = u32x4{0, 0, 0, 0};
  SCHED();

  sfor<0, 20>([&](auto S) {
    constexpr int s = decltype(S)::value;
    if constexpr (s + 1 < 20) {
      constexpr int pend = pend_at(s);
      asm volatile("s_waitcnt vmcnt(%0) lgkmcnt(0)" ::"i"(pend) : "memory");
      BAR();
    }
    sfor<0, 5>([&](auto T) {
      constexpr int t = decltype(T)::value;
      sfor<0, 4>([&](auto I) {
        constexpr int i = decltype(I)::value;
        { constexpr int n = 4 * t + i, tt = n % 5, ii = n / 5; mfma16a(acc[tt][ii], fa[s & 1][tt], fb[s & 1][ii]); }
        if constexpr (s + 1 < 20) {
          if constexpr (t == 0) readB(IC<s + 1>{}, I);
          if constexpr (t >= 1 && (i & 1) == 0 && 2 * (t - 1) + (i >> 1) < 5) readA(IC<s + 1>{}, IC<2 * (t - 1) + (i >> 1)>{});
        }
        if constexpr (i == 3 && t < 2) issue_x(IC<s / 2 + 2>{}, IC<2 * (s & 1) + t>{});
        if constexpr (i == 3 && (t == 2 || t == 3)) issue_w(IC<s + 3>{}, IC<t - 2>{});
        if constexpr (t == 3 && i == 2 && s + 1 < 20) fc[(s + 1) & 1] = trread2(L + cb + ((s + 1) % NWS) * WSTG, 640);
        if constexpr (t == 2 && i == 1) mfma16a(csa, fc[s & 1], ONES4);
        SCHED();
      });
    });
    if ((s & 3) == tq) mfma16a(csb, fa[s & 1][4], ONES4);
    if constexpr (s == 1) {
      relane();
      pinu(xr0); pinu(xr1);
      float v[16];
#pragma unroll
      for (int j = 0; j < 4; ++j) {
        v[2 * j] = __uint_as_float(xr0[j] << 16); v[2 * j + 1] = __uint_as_float(xr0[j] & 0xffff0000u);
        v[8 + 2 * j] = __uint_as_float(xr1[j] << 16); v[9 + 2 * j] = __uint_as_float(xr1[j] & 0xffff0000u);
      }
      const bool ex = l < 16;
      float a = 0.f, c = 0.f;
#pragma unroll
      for (int j = 0; j < 8; ++j) { a += v[j]; c += v[8 + j]; }
      const float mean = wave_sum(a + (ex ? c : 0.f)) * (1.f / 640.f);
      a = 0.f; c = 0.f;
#pragma unroll
      for (int j = 0; j < 8; ++j) { const float d0 = v[j] - mean, d1 = v[8 + j] - mean; a = fmaf(d0, d0, a); c = fmaf(d1, d1, c); }
      const float var = wave_sum(a + (ex ? c : 0.f)) * (1.f / 640.f);
      gst(ws + OFF_ST + (size_t)gw * 16, u32x4{__float_as_uint(-mean), tag, __float_as_uint(var + 1e-5f), tag});
    }
    if constexpr (s == 0) { relane(); dma4(warm_addr(gw * 11 + min(l, 10), 2, x_, wqkv_, wg_, wu_), lb + L_DUMMY); }
    if constexpr (s == 1) { relane(); dma4(warm_addr2(gw * 11 + min(l, 10), wd_, wo_), lb + L_DUMMY); }
    if constexpr (s == S_SIDE) { relane(); side_dma(); ld_shared(); }
#define ST_A 4
#define ST_B 9
#define ST_C 14
    if constexpr (s == ST_A) STAMP(0, 3);
    if constexpr (s == ST_B) STAMP(0, 4);
    if constexpr (s == ST_C) STAMP(0, 5);
    SCHED();
  });
  asm volatile("s_nop 7\n\ts_nop 7" ::: "memory");
  SCHED();
  STAMP(0, 6);
  relane();
  WAIT_V(0);
  if (__builtin_expect(sh_bad(), 0)) {
    unsigned spin = 0;
    do {
      __builtin_amdgcn_s_sleep(4);
      ld_shared();
    } while (sh_bad() && ++spin < (1u << 16));
  }
  if (tok == 0) {
    *(LAS f32x4*)(L + L_CSS + (fgi * 128 + tq * 16 + 4 * g) * 4) = csa;
    *(LAS f32x4*)(L + L_CSS + (fgi * 128 + 64 + tq * 16 + 4 * g) * 4) = csb;
  }
  __syncthreads();
  float nmean[4], vare[4], rstd[4];
#pragma unroll
  for (int i = 0; i < 4; ++i) {
    nmean[i] = __uint_as_float(stv[i][0]); vare[i] = __uint_as_float(stv[i][2]);
    rstd[i] = __builtin_amdgcn_rsqf(vare[i]);
  }
#pragma unroll
  for (int t = 0; t < 5; ++t) {
    f32x4 c4 = *(LAS f32x4*)(L + L_CSS + (fgi * 128 + t * 16 + 4 * g) * 4);
    if (t == 4) {
#pragma unroll
      for (int k = 1; k < 4; ++k) c4 += *(LAS f32x4*)(L + L_CSS + (fgi * 128 + 64 + k * 16 + 4 * g) * 4);
    }
#pragma unroll
    for (int i = 0; i < 4; ++i)
#pragma unroll
      for (int c = 0; c < 4; ++c) acc[t][i][c] = fmaf(nmean[i], c4[c], acc[t][i][c]);
  }
  STAMP(0, 7);
  if (bt >= 2) {
#pragma unroll
    for (int i = 0; i < 3; ++i) {
      const u32x4 v = trread2(L + L_PT + (w * 3 + i) * 1024 + (8 * g + q) * 32 + 8 * p, 128);
      if (mw + 1024 * i < 2720) gst(ws + OFF_WT + (size_t)(mw + 1024 * i) * 1024 + l * 16, v);
    }
    const u32x4 sent = u32x4{0xFFFFFFFFu, 0xFFFFFFFFu, 0xFFFFFFFFu, 0xFFFFFFFFu};
    gst(ws + OFF_O + (size_t)mb * 20480 + tid * 16, sent);
    gst(ws + OFF_O + (size_t)mb * 20480 + 8192 + tid * 16, sent);
    if (tid < 256) gst(ws + OFF_O + (size_t)mb * 20480 + 16384 + tid * 16, sent);
    if (mb < 105 && tid < 32) gst(ws + OFF_KVT + mb * 512 + tid * 16, sent);
  }
  const int tile0 = tg * 16 + tq * 4;

  if (kind == 3) {
    const int hi = l >> 5, pp = g & 1;
#pragma unroll
    for (int i = 0; i < 2; ++i) {
      const float rs_ = hi ? rstd[i + 2] : rstd[i], ve = hi ? vare[i + 2] : vare[i];
      const float c1 = -2.0f * 0.7978845608028654f * 1.4426950408889634f;
      const float cA = c1 * rs_, cB = c1 * 0.044715f * rs_ * rs_ * rs_;
      unsigned hp[10];
#pragma unroll
      for (int t = 0; t < 5; ++t) {
#pragma unroll
        for (int c2 = 0; c2 < 2; ++c2) {
          f32x2 gg, uu;
#pragma unroll
          for (int c = 0; c < 2; ++c) {
            const float va = acc[t][i][2 * c2 + c], vb = acc[t][i + 2][2 * c2 + c];
            const auto sw = __builtin_amdgcn_permlane32_swap(__float_as_uint(va), __float_as_uint(vb), false, false);
            const unsigned s0 = sw[0], s1 = sw[1];
            gg[c] = __uint_as_float(s0); uu[c] = __uint_as_float(s1);
          }
          const f32x2 arg = __builtin_elementwise_fma(gg * gg, f32x2{cB, cB}, f32x2{cA, cA}) * gg;
          const f32x2 e = f32x2{__builtin_amdgcn_exp2f(arg[0]), __builtin_amdgcn_exp2f(arg[1])};
          const f32x2 dn = __builtin_elementwise_fma(e, f32x2{ve, ve}, f32x2{ve, ve});
          const f32x2 rr = f32x2{__builtin_amdgcn_rcpf(dn[0]), __builtin_amdgcn_rcpf(dn[1])};
          const f32x2 hv = gg * uu * rr;
          hp[2 * t + c2] = pk2(hv[0], hv[1]);
        }
      }
      const int tl = tile0 + ((i + 2 * hi + 2 * fgi) & 3);
      char* dst = ws + OFF_H + (size_t)tl * 49152 + (mbase >> 3) * 256 + tok * 16;
      if (pp == 0) {
        gst(dst, u32x4{hp[0], hp[1], hp[2], hp[3]});
        gst(dst + 256, u32x4{hp[4], hp[5], hp[6], hp[7]});
        gst(dst + 512, u32x2{hp[8], hp[9]});
      } else {
        gst(dst + 520, u32x2{hp[0], hp[1]});
        gst(dst + 768, u32x4{hp[2], hp[3], hp[4], hp[5]});
        gst(dst + 1024, u32x4{hp[6], hp[7], hp[8], hp[9]});
      }
    }
  } else {
    u32x4 bq[4][3];
    if (kind != 2) {
#pragma unroll
      for (int i = 0; i < 4; ++i) {
#pragma unroll
        for (int s = 0; s < 2; ++s)
          bq[i][s] = u32x4{pk2(acc[2 * s][i][0], acc[2 * s][i][1]), pk2(acc[2 * s][i][2], acc[2 * s][i][3]),
                           pk2(acc[2 * s + 1][i][0], acc[2 * s + 1][i][1]), pk2(acc[2 * s + 1][i][2], acc[2 * s + 1][i][3])};
        bq[i][2] = u32x4{pk2(acc[4][i][0], acc[4][i][1]), pk2(acc[4][i][2], acc[4][i][3]), 0u, 0u};
      }
    }
    if (bt == 0) {
      LAS char* xch = L + L_PK + (64 * tq + 32) * PK_PITCH + l * 16;
      if (kind == 1) {
#pragma unroll
        for (int j = 0; j < 6; ++j) *(LAS u32x4*)(xch + j * 1024) = bq[2 + j / 3][j % 3];
      } else {
#pragma unroll
        for (int i = 0; i < 4; ++i) {
          const int trow = 64 * tq + 16 * ((i + 2) & 3) + tok;
#pragma unroll
          for (int t = 0; t < 5; ++t)
            *(LAS u32x2*)(L + L_V + trow * V_PITCH + 40 * g + 8 * t) =
                u32x2{pk2(acc[t][i][0] * rstd[i], acc[t][i][1] * rstd[i]), pk2(acc[t][i][2] * rstd[i], acc[t][i][3] * rstd[i])};
          if (g == 0) *(LAS u32x2*)(L + L_V + trow * V_PITCH + 160) = u32x2{0x00003F80u, 0u};
        }
      }
      __syncthreads();
      if (kind == 2) {
#pragma unroll
        for (int j = 0; j < 6; ++j) bq[j / 3][j % 3] = *(LAS u32x4*)(xch + j * 1024);
      }
    }
    const int wb = (p == g) ? (L_WPHI + g * 800 + q * 40) : L_ZERO;
    auto phi2 = [&](auto I0) {
      constexpr int i0 = decltype(I0)::value;
      f32x4 pq[5][2];
#pragma unroll
      for (int tt = 0; tt < 5; ++tt) {
        const u32x4 a0 = trread2(L + wb + 8 * tt, 160), a1 = trread2(L + wb + 8 * tt + 320, 160);
        const u32x2 a2l = trread(L + wb + 8 * tt + 640);
        const u32x4 a2 = u32x4{a2l[0], a2l[1], 0u, 0u};
#pragma unroll
        for (int i = 0; i < 2; ++i) {
          f32x4 o = f32x4{0.f, 0.f, 0.f, 0.f};
          o = mfma16(a0, bq[i0 + i][0], o);
          o = mfma16(a1, bq[i0 + i][1], o);
          o = mfma16(a2, bq[i0 + i][2], o);
          pq[tt][i] = o;
        }
      }
#pragma unroll
      for (int i = 0; i < 2; ++i) {
        const float c1 = rstd[i0 + i] * 1.4426950408889634f;
        float mx = pq[0][i][0], mn = pq[0][i][0];
#pragma unroll
        for (int f = 1; f < 20; ++f) { mx = fmaxf(mx, pq[f >> 2][i][f & 3]); mn = fminf(mn, pq[f >> 2][i][f & 3]); }
        const float bx = -mx * c1, bn = mn * c1;
        f32x2 ep[10], en[10], sp2 = f32x2{0.f, 0.f}, sn2 = f32x2{0.f, 0.f};
#pragma unroll
        for (int f = 0; f < 10; ++f) {
          const f32x2 pv = f32x2{pq[f >> 1][i][2 * (f & 1)], pq[f >> 1][i][2 * (f & 1) + 1]};
          const f32x2 ap = __builtin_elementwise_fma(pv, f32x2{c1, c1}, f32x2{bx, bx});
          const f32x2 an = __builtin_elementwise_fma(pv, f32x2{-c1, -c1}, f32x2{bn, bn});
          ep[f] = f32x2{__builtin_amdgcn_exp2f(ap[0]), __builtin_amdgcn_exp2f(ap[1])};
          en[f] = f32x2{__builtin_amdgcn_exp2f(an[0]), __builtin_amdgcn_exp2f(an[1])};
          sp2 += ep[f]; sn2 += en[f];
        }
        const float rp = __builtin_amdgcn_rcpf(sp2[0] + sp2[1]), rn = __builtin_amdgcn_rcpf(sn2[0] + sn2[1]);
        unsigned o[20];
#pragma unroll
        for (int f = 0; f < 10; ++f) {
          const f32x2 a = ep[f] * f32x2{rp, rp}, c = en[f] * f32x2{rn, rn};
          o[f] = pk2(a[0], a[1]);
          o[10 + f] = pk2(c[0], c[1]);
        }
        if (kind == 0) {
          char* dst = ws + OFF_PQ + (size_t)((tile0 + i0 + i) * 8 + ga) * 5120 + tok * 16;
#pragma unroll
          for (int ci = 0; ci < 4; ++ci) gst(dst + g * 1024 + ci * 256, u32x4{o[4 * ci], o[4 * ci + 1], o[4 * ci + 2], o[4 * ci + 3]});
          gst(dst + 4096 + g * 256, u32x4{o[16], o[17], o[18], o[19]});
        } else {
          const int trow = 64 * tq + 32 * fgi + 16 * i + tok;
#pragma unroll
          for (int ci = 0; ci < 5; ++ci)
            *(LAS u32x4*)(L + L_PK + trow * PK_PITCH + g * 80 + ci * 16) = u32x4{o[4 * ci], o[4 * ci + 1], o[4 * ci + 2], o[4 * ci + 3]};
        }
      }
    };
    phi2(IC<0>{});
    if (kind == 0) phi2(IC<2>{});
  }
  if (bt == 0) {
    __syncthreads();
    const int e = w >> 1, bh = w & 1;
    const int rowoff = 8 * g + q;
    LAS char* pa = L + L_PK + rowoff * PK_PITCH + e * 80 + 8 * p;
    LAS char* pa2 = L + L_PK + rowoff * PK_PITCH + e * 80 + 64 + 8 * (p & 1);
    LAS char* pb = L + L_V + rowoff * V_PITCH + (bh == 0 ? e * 40 + 8 * p : (p == 0 ? e * 40 + 32 : 160));
    f32x4 d[3];
#pragma unroll
    for (int k = 0; k < 3; ++k) d[k] = f32x4{0.f, 0.f, 0.f, 0.f};
#pragma unroll
    for (int ks = 0; ks < 8; ++ks) {
      const u32x4 bb = trread2(pb + ks * 32 * V_PITCH, 4 * V_PITCH);
      d[0] = mfma16(trread2(pa + ks * 32 * PK_PITCH, 4 * PK_PITCH), bb, d[0]);
      d[1] = mfma16(trread2(pa + ks * 32 * PK_PITCH + 32, 4 * PK_PITCH), bb, d[1]);
      d[2] = mfma16(trread2(pa2 + ks * 32 * PK_PITCH, 4 * PK_PITCH), bb, d[2]);
    }
    const int dcol = 16 * bh + tok;
    if (dcol <= 20) {
      float* dst = (float*)(ws + OFF_KVP) + ((size_t)(tg * 32 + 4 * ga + e) * 21 + dcol) * 40 + 4 * g;
      *(f32x4*)(dst) = d[0];
      *(f32x4*)(dst + 16) = d[1];
      if (g < 2) *(f32x4*)(dst + 32) = d[2];
    }
  }
  asm volatile("" ::"v"(cpf));
  STAMP(0, 8);
  WAIT_V(0);
  STAMP_FLUSH(0);
}

constexpr int K2_LDS = 163840;
__global__ __launch_bounds__(512, 2) void k2_out(const __hip_bfloat16* __restrict__ x_, char* __restrict__ ws,
                                                 __hip_bfloat16* __restrict__ out_) {
  extern __shared__ __attribute__((aligned(16))) char smem[];
  LAS char* L = (LAS char*)smem;
  const int tid = threadIdx.x, l = tid & 63;
  const int w = __builtin_amdgcn_readfirstlane(tid >> 6);
  const int b = blockIdx.x, xcd = b & 7, idx = b >> 3;
  STAMP_DECL;
  STAMP(1, 0);
  const __amdgpu_buffer_rsrc_t rs = __builtin_amdgcn_make_buffer_rsrc((void*)ws, 0, (int)WS_BYTES, 0x00020000);
  const bool red = (w == 7 && b < 210);
  f32x2 pv[NP];
  if (w == 7) __builtin_amdgcn_s_setprio(3);
  if (red) {
    const GAS f32x2* pp = (const GAS f32x2*)(ws + OFF_KVP) + b * 64 + l;
#pragma unroll
    for (int k = 0; k < NP; ++k) pv[k] = pp[k * 13440];
  }
  SCHED();
  BAR();
  const unsigned cpf = code_prefetch<K2_PF>(idx, tid);
  const int tg = xcd * 4 + (idx & 3);
  const int fg = idx >> 2;
  const int vo = l * 16;
  const int so_h = (int)OFF_H + tg * 4 * 49152 + 6 * w * 1024;
  const int so_a = (int)OFF_WT + fg * 68 * 5120 + 6 * w * 5120;
  const int ok0 = w < 4 ? 2 * w : 3 * w - 4;
  const int so_o = (int)OFF_WT + fg * 68 * 5120 + (48 + ok0) * 5120;
  const int so_f = (int)OFF_O + (tg * 80 + ok0) * 1024;
  auto ldg = [&](int so) -> u32x4 { return __builtin_amdgcn_raw_buffer_load_b128(rs, vo, so, 0); };
  f32x4 acc[5][4];
#pragma unroll
  for (int nt = 0; nt < 5; ++nt)
#pragma unroll
    for (int i = 0; i < 4; ++i) { acc[nt][i] = f32x4{0.f, 0.f, 0.f, 0.f}; pin4(acc[nt][i]); }

  u32x4 fa[6][5], fb[6][4], ca[3][5], ob[3][4];
  auto ld = [&](auto K) {
    constexpr int k = decltype(K)::value;
#pragma unroll
    for (int i = 0; i < 4; ++i) fb[k][i] = ldg(so_h + i * 49152 + k * 1024);
#pragma unroll
    for (int nt = 0; nt < 5; ++nt) fa[k][nt] = ldg(so_a + k * 5120 + nt * 1024);
    SCHED();
  };
  auto mm = [&](auto K) {
    constexpr int k = decltype(K)::value;
#pragma unroll
    for (int nt = 0; nt < 5; ++nt)
#pragma unroll
      for (int i = 0; i < 4; ++i) mfma16a(acc[nt][i], fa[k][nt], fb[k][i]);
    SCHED();
  };
  auto ldo = [&](int s) {
#pragma unroll
    for (int nt = 0; nt < 5; ++nt) ca[s][nt] = ldg(so_o + s * 5120 + nt * 1024);
    SCHED();
  };
  auto ldf = [&](int s) {
#pragma unroll
    for (int i = 0; i < 4; ++i) ob[s][i] = __builtin_amdgcn_raw_buffer_load_b128(rs, vo, so_f + i * 20480 + s * 1024, 16);
  };
  auto fbad = [&](int s) -> bool {
    unsigned mxw = 0u;
#pragma unroll
    for (int i = 0; i < 4; ++i)
#pragma unroll
      for (int j = 0; j < 4; ++j) mxw = max(mxw, ob[s][i][j]);
    return __any(mxw == 0xFFFFFFFFu);
  };
  auto ostep = [&](int s) {
    if (__builtin_expect(fbad(s), 0)) {
      unsigned spin = 0;
      do {
        __builtin_amdgcn_s_sleep(4);
        ldf(s);
      } while (fbad(s) && ++spin < (1u << 14));
    }
#pragma unroll
    for (int nt = 0; nt < 5; ++nt)
#pragma unroll
      for (int i = 0; i < 4; ++i) mfma16a(acc[nt][i], ca[s][nt], ob[s][i]);
    SCHED();
  };

  if (red) {
    f32x2 s = pv[0];
#pragma unroll
    for (int k = 1; k < NP; ++k) s += pv[k];
    __builtin_amdgcn_raw_buffer_store_b32(pk2(s[0], s[1]), rs, (int)OFF_KVT + b * 256 + l * 4, 0, 16);
  }
  __builtin_amdgcn_s_setprio(3);
  SCHED();
  STAMP(1, 1);
  ld(IC<0>{}); ld(IC<1>{}); ld(IC<2>{});
  __builtin_amdgcn_s_setprio(2);
  mm(IC<0>{}); ld(IC<3>{});
  asm volatile("" ::"v"(cpf));
  mm(IC<1>{});
  if (w < 4) {
    const int r = l & 15, g = l >> 4;
    const int row1 = (r & 3) == 0 ? 16 + (r >> 2) : 20;
    const int so_k = (int)OFF_KVT + 4 * fg * 1680;
    const int ko0 = r * 80 + g * 16, ko1 = row1 * 80 + g * 16, kt0 = r * 80 + g * 1680 + 64, kt1 = row1 * 80 + g * 1680 + 64;
    u32x4 pf[5], ka[4][2], kt[2];
#pragma unroll
    for (int f = 0; f < 5; ++f) pf[f] = ldg((int)OFF_PQ + ((tg * 4 + w) * 8 + fg) * 5120 + f * 1024);
    for (unsigned spin = 0; spin < (1u << 14); ++spin) {
#pragma unroll
      for (int hd = 0; hd < 4; ++hd) {
        ka[hd][0] = __builtin_amdgcn_raw_buffer_load_b128(rs, ko0, so_k + hd * 1680, 16);
        ka[hd][1] = __builtin_amdgcn_raw_buffer_load_b128(rs, ko1, so_k + hd * 1680, 16);
      }
      kt[0] = __builtin_amdgcn_raw_buffer_load_b128(rs, kt0, so_k, 16);
      kt[1] = __builtin_amdgcn_raw_buffer_load_b128(rs, kt1, so_k, 16);
      unsigned mxw = 0u;
#pragma unroll
      for (int k = 0; k < 2; ++k)
#pragma unroll
        for (int j = 0; j < 4; ++j) {
          mxw = max(mxw, kt[k][j]);
#pragma unroll
          for (int hd = 0; hd < 4; ++hd) mxw = max(mxw, ka[hd][k][j]);
        }
      if (!__any(mxw == 0xFFFFFFFFu)) break;
      __builtin_amdgcn_s_sleep(4);
    }
    float ov[20];
#pragma unroll
    for (int hd = 0; hd < 4; ++hd) {
      const u32x4 pt = (g == hd) ? pf[4] : u32x4{0u, 0u, 0u, 0u};
      f32x4 o0 = f32x4{0.f, 0.f, 0.f, 0.f}, o1 = f32x4{0.f, 0.f, 0.f, 0.f};
      o0 = mfma16(ka[hd][0], pf[hd], o0);
      o1 = mfma16(ka[hd][1], pf[hd], o1);
      o0 = mfma16(kt[0], pt, o0);
      o1 = mfma16(kt[1], pt, o1);
      const float inv = __builtin_amdgcn_rcpf(o1[1] + 1e-6f);
#pragma unroll
      for (int v = 0; v < 4; ++v) ov[5 * hd + v] = o0[v] * inv;
      ov[5 * hd + 4] = o1[0] * inv;
    }
    unsigned op[10];
#pragma unroll
    for (int j = 0; j < 10; ++j) op[j] = pk2(ov[2 * j], ov[2 * j + 1]);
    const int so_d = (int)OFF_O + ((tg * 4 + w) * 20 + (fg >> 1) * 5) * 1024;
    if ((fg & 1) == 0) {
      __builtin_amdgcn_raw_buffer_store_b128(u32x4{op[0], op[1], op[2], op[3]}, rs, vo, so_d, 16);
      __builtin_amdgcn_raw_buffer_store_b128(u32x4{op[4], op[5], op[6], op[7]}, rs, vo, so_d + 1024, 16);
      __builtin_amdgcn_raw_buffer_store_b64(u32x2{op[8], op[9]}, rs, vo, so_d + 2048, 16);
    } else {
      __builtin_amdgcn_raw_buffer_store_b64(u32x2{op[0], op[1]}, rs, vo + 8, so_d + 2048, 16);
      __builtin_amdgcn_raw_buffer_store_b128(u32x4{op[2], op[3], op[4], op[5]}, rs, vo, so_d + 3072, 16);
      __builtin_amdgcn_raw_buffer_store_b128(u32x4{op[6], op[7], op[8], op[9]}, rs, vo, so_d + 4096, 16);
    }
  }
  SCHED();
  STAMP(1, 2);
  ld(IC<4>{});
  __builtin_amdgcn_s_setprio(1);
  mm(IC<2>{}); ld(IC<5>{});
  mm(IC<3>{}); ldo(0); ldo(1);
  __builtin_amdgcn_s_setprio(0);
  mm(IC<4>{}); ldf(0); ldf(1); SCHED();
  if (w >= 4) { ldo(2); ldf(2); }
  SCHED();
  u32x2 xv[3];
#pragma unroll
  for (int k = 0; k < 3; ++k) {
    const int t = min(w + 8 * k, 19), nt = t >> 2, i = t & 3;
    xv[k] = *(const u32x2*)((const char*)x_ + ((size_t)(tg * 64 + i * 16 + (l & 15)) * 640 + fg * 80 + nt * 16 + 4 * (l >> 4)) * 2);
  }
  SCHED();
  mm(IC<5>{});
  STAMP(1, 3);
  ostep(0);
  ostep(1);
  if (w >= 4) ostep(2);
  SCHED();
  asm volatile("s_nop 7\n\ts_nop 7" ::: "memory");
  SCHED();
  STAMP(1, 4);
#pragma unroll
  for (int nt = 0; nt < 5; ++nt)
#pragma unroll
    for (int i = 0; i < 4; ++i) *(LAS f32x4*)(L + ((w * 20 + nt * 4 + i) * 64 + l) * 16) = acc[nt][i];
  __syncthreads();
  STAMP(1, 5);
#pragma unroll
  for (int k = 0; k < 3; ++k) {
    const int t = w + 8 * k;
    if (t < 20) {
      f32x4 s = *(LAS f32x4*)(L + (t * 64 + l) * 16);
#pragma unroll
      for (int ww = 1; ww < 8; ++ww) s += *(LAS f32x4*)(L + ((ww * 20 + t) * 64 + l) * 16);
      const int nt = t >> 2, i = t & 3;
      const size_t off = ((size_t)(tg * 64 + i * 16 + (l & 15)) * 640 + fg * 80 + nt * 16 + 4 * (l >> 4)) * 2;
      const float x0 = __builtin_bit_cast(float, xv[k][0] << 16), x1 = __builtin_bit_cast(float, xv[k][0] & 0xffff0000u);
      const float x2 = __builtin_bit_cast(float, xv[k][1] << 16), x3 = __builtin_bit_cast(float, xv[k][1] & 0xffff0000u);
      *(u32x2*)((char*)out_ + off) = u32x2{pk2(s[0] + x0, s[1] + x1), pk2(s[2] + x2, s[3] + x3)};
    }
  }
  STAMP(1, 6);
  STAMP_FLUSH(1);
}

extern "C" void kernel_launch(void* const* d_in, const int* in_sizes, int n_in, void* d_out, int out_size, void* d_ws,
                              size_t ws_size, hipStream_t stream) {
  const __hip_bfloat16* x = (const __hip_bfloat16*)d_in[0];
  const __hip_bfloat16* wqkv = (const __hip_bfloat16*)d_in[1];
  const __hip_bfloat16* wphi = (const __hip_bfloat16*)d_in[2];
  const __hip_bfloat16* wo = (const __hip_bfloat16*)d_in[3];
  const __hip_bfloat16* wg = (const __hip_bfloat16*)d_in[5];
  const __hip_bfloat16* wu = (const __hip_bfloat16*)d_in[6];
  const __hip_bfloat16* wd = (const __hip_bfloat16*)d_in[7];
  (void)hipFuncSetAttribute((const void*)k1_proj, hipFuncAttributeMaxDynamicSharedMemorySize, K1_LDS);
  (void)hipFuncSetAttribute((const void*)k2_out, hipFuncAttributeMaxDynamicSharedMemorySize, K2_LDS);
  k1_proj<<<256, 512, K1_LDS, stream>>>(x, wqkv, wphi, wo, wg, wu, wd, (char*)d_ws);
  k2_out<<<256, 512, K2_LDS, stream>>>(x, (char*)d_ws, (__hip_bfloat16*)d_out);
}
```

```cpp
#include <hip/hip_runtime.h>
#include <hip/hip_bf16.h>
#include <stdint.h>

#define GAS __attribute__((address_space(1)))
#define LAS __attribute__((address_space(3)))
#define DI __device__ __forceinline__
typedef __bf16 bf16x8 __attribute__((ext_vector_type(8)));
typedef __bf16 bf16x2 __attribute__((ext_vector_type(2)));
typedef short s16x4 __attribute__((ext_vector_type(4)));
typedef float f32x4 __attribute__((ext_vector_type(4)));
typedef float f32x2 __attribute__((ext_vector_type(2)));
typedef unsigned u32x4 __attribute__((ext_vector_type(4)));
typedef unsigned u32x2 __attribute__((ext_vector_type(2)));

extern "C" __device__ unsigned long long llvm_dispatch_id() __asm("llvm.amdgcn.dispatch.id");
template <int N> struct IC { static constexpr int value = N; };
template <int B, int E, class F> DI void sfor(F&& f) {
  if constexpr (B < E) { f(IC<B>{}); sfor<B + 1, E>(f); }
}

constexpr unsigned OFF_H = 0;
constexpr unsigned OFF_PQ = 6291456;
constexpr unsigned OFF_WT = 11534336;
constexpr unsigned OFF_KVP = 14483456;
constexpr unsigned OFF_KVT = 15343616;
constexpr unsigned OFF_ST = 15397376;
constexpr unsigned OFF_O = OFF_ST + 32768;
constexpr unsigned WS_BYTES = OFF_O + 2621440;
constexpr int NP = 8;

constexpr int SUB = 2688;
constexpr int WREG = SUB + 2560;
constexpr int WSTG = 2 * WREG;
constexpr int XSTG = 32768;
constexpr int NXS = 3, NWS = 4;
constexpr int L_WPHI = 0;
constexpr int L_ZERO = L_WPHI + 4096;
constexpr int L_ST = L_ZERO + 1024;
constexpr int L_CSS = L_ST + 2048;
constexpr int L_DUMMY = L_CSS + 2048;
constexpr int L_RING = L_CSS + 2560;
constexpr int L_XR = L_RING, L_WR = L_XR + NXS * XSTG;
constexpr int L_PT = L_XR + XSTG;
constexpr int K1_LDS = L_WR + NWS * WSTG;
constexpr int PK_PITCH = 336, V_PITCH = 176;
constexpr int L_PK = L_RING, L_V = L_PK + 256 * PK_PITCH;

DI unsigned pk2(float a, float b) {
  bf16x2 v; v[0] = (__bf16)a; v[1] = (__bf16)b;
  return __builtin_bit_cast(unsigned, v);
}
DI u32x2 trread(LAS char* p) {
  s16x4 v = __builtin_amdgcn_ds_read_tr16_b64_v4i16((LAS s16x4*)p);
  return __builtin_bit_cast(u32x2, v);
}
DI u32x4 trread2(LAS char* p, int hi) {
  u32x2 a = trread(p), b = trread(p + hi);
  return u32x4{a[0], a[1], b[0], b[1]};
}
DI f32x4 mfma16(u32x4 a, u32x4 b, f32x4 c) {
  return __builtin_amdgcn_mfma_f32_16x16x32_bf16(__builtin_bit_cast(bf16x8, a), __builtin_bit_cast(bf16x8, b), c, 0, 0, 0);
}
DI void mfma16a(f32x4& c, const u32x4& a, const u32x4& b) {
  asm volatile("v_mfma_f32_16x16x32_bf16 %0, %1, %2, %0" : "+v"(c) : "v"(a), "v"(b));
}
DI void gload16a(u32x4& dst, unsigned voff, const char* sbase) {
  asm volatile("global_load_dwordx4 %0, %1, %2" : "=v"(dst) : "v"(voff), "s"(sbase));
}
template <int CTRL, int RM> DI float dpp_add(float v) {
  return v + __int_as_float(__builtin_amdgcn_update_dpp(0, __float_as_int(v), CTRL, RM, 0xf, false));
}
DI float wave_sum(float v) {
  v = dpp_add<0x111, 0xf>(v);
  v = dpp_add<0x112, 0xf>(v);
  v = dpp_add<0x114, 0xf>(v);
  v = dpp_add<0x118, 0xf>(v);
  v = dpp_add<0x142, 0xa>(v);
  v = dpp_add<0x143, 0xc>(v);
  return __int_as_float(__builtin_amdgcn_readlane(__float_as_int(v), 63));
}
DI void dma4(const void* g, int ldsaddr) {
  asm volatile("s_mov_b32 m0, %1\n\ts_nop 0\n\tglobal_load_lds_dword %0, off" ::"v"(g), "s"(ldsaddr) : "memory");
}
template <int OFF = 0> DI void dma16s(unsigned voff, const char* sbase, int ldsaddr) {
  asm volatile("s_mov_b32 m0, %2\n\ts_nop 0\n\tglobal_load_lds_dwordx4 %0, %1 offset:%3" ::"v"(voff), "s"(sbase), "s"(ldsaddr - OFF), "i"(OFF) : "memory");
}
DI void dma16s_q(unsigned voff, const char* sbase, int ldsaddr) {
  asm volatile("s_mov_b32 m0, %2\n\ts_mov_b64 exec, 0xffff\n\tglobal_load_lds_dwordx4 %0, %1\n\ts_mov_b64 exec, -1" ::"v"(voff), "s"(sbase),
               "s"(ldsaddr)
               : "memory");
}
DI int opaque(int v) { asm volatile("" : "+v"(v)); return v; }
DI void pin4(f32x4& v) { asm volatile("" : "+v"(v)); }
DI void pinu(u32x4& v) { asm volatile("" : "+v"(v)); }
DI void gst(char* p, u32x4 v) { asm volatile("global_store_dwordx4 %0, %1, off sc1\n\ts_nop 1" ::"v"(p), "v"(v) : "memory"); }
DI void gst(char* p, u32x2 v) { asm volatile("global_store_dwordx2 %0, %1, off sc1\n\ts_nop 1" ::"v"(p), "v"(v) : "memory"); }
#define WAIT_V(n) asm volatile("s_waitcnt vmcnt(%0)" ::"i"(n) : "memory")
#define STAMP_DECL
#define STAMP(k, i)
#define STAMP_FLUSH(k)
template <int NL> DI unsigned code_prefetch(int idx, int tid) {
  unsigned long long pc;
  asm volatile("s_getpc_b64 %0" : "=s"(pc));
  unsigned v = 0;
  if (tid < NL) v = *(const GAS unsigned*)(pc + (unsigned long long)(idx * NL + tid) * 64);
  return v;
}
DI const char* uptr(const char* p) {
  const unsigned long long v = (unsigned long long)p;
  const unsigned lo = __builtin_amdgcn_readfirstlane((unsigned)v), hi = __builtin_amdgcn_readfirstlane((unsigned)(v >> 32));
  return (const char*)(((unsigned long long)hi << 32) | lo);
}
#define BAR() __builtin_amdgcn_s_barrier()
#define SCHED() __builtin_amdgcn_sched_barrier(0)
#define K1_PF 15
#define K2_PF 3

constexpr int NB = 1;
constexpr int S_SIDE = 16;
constexpr int xtra(int s) { return (s < 1 ? NB : 0) + (s == 1 ? 1 : 0) + (s == S_SIDE ? 1 + 4 : 0); }
constexpr int pend_at(int s) {
  int n = 0;
  auto op = [&](int cnt, int need) { if (need <= s + 1) n = 0; else n += cnt; };
  op(4, 0); op(2, 0); op(NB, 99); op(2, 1); op(NB + 2, 99); op(4, 2); op(2, 2); op(NB, 99);
  for (int st = 0; st < s; ++st) {
    if (st / 2 + 2 < 10) op(2, 2 * (st / 2 + 2));
    if (st + 3 < 20) op(2, st + 3);
    op(xtra(st), 99);
  }
  return n;
}
DI const char* warm_addr(int n, int j, const void* px, const void* pq, const void* pg, const void* pu) {
  n = min(n, 21119);
  const int xr = n / 3, xl = n - 3 * xr;
  const int m = max(n - 6144, 0), r = m / 78, c = m - 78 * r;
  const unsigned k = 64 + 192 * j + r;
  unsigned long long a = (unsigned long long)pq + k * 3840u + c * 128u;
  a = c >= 30 ? (unsigned long long)pg + k * 3072u + (c - 30) * 128u : a;
  a = c >= 54 ? (unsigned long long)pu + k * 3072u + (c - 54) * 128u : a;
  a = n < 6144 ? (unsigned long long)px + xr * 1280u + (1 + 3 * j + xl) * 128u : a;
  return (const char*)a;
}
DI const char* warm_addr2(int n, const void* pd, const void* po) {
  n = min(n, 21759);
  return (const char*)((n < 15360 ? (unsigned long long)pd : (unsigned long long)po - 15360ull * 128) + (unsigned long long)n * 128);
}

__global__ __launch_bounds__(512, 2) void k1_proj(const __hip_bfloat16* __restrict__ x_, const __hip_bfloat16* __restrict__ wqkv_,
                                                  const __hip_bfloat16* __restrict__ wphi_, const __hip_bfloat16* __restrict__ wo_,
                                                  const __hip_bfloat16* __restrict__ wg_, const __hip_bfloat16* __restrict__ wu_,
                                                  const __hip_bfloat16* __restrict__ wd_, char* __restrict__ ws) {
  extern __shared__ __attribute__((aligned(16))) char smem[];
  LAS char* L = (LAS char*)smem;
  const int lb = (int)(size_t)L;
  STAMP_DECL;
  STAMP(0, 0);
  int tid = threadIdx.x, l = tid & 63;
  const int w = __builtin_amdgcn_readfirstlane(tid >> 6);
  const int tq = w & 3, fgi = w >> 2;
  const int b = blockIdx.x, xcd = b & 7, idx = b >> 3;
  const unsigned cpf = code_prefetch<K1_PF>(idx, tid);
  const int tg = (xcd & 1) * 4 + (idx & 3);
  const int cg = (xcd >> 1) * 8 + (idx >> 2);
  const int ga = cg >> 2, bt = cg & 3;
  const int kind = bt == 0 ? 1 + fgi : ((bt == 1 && fgi == 0) ? 0 : 3);
  const int mj = bt == 1 ? ga : 8 + 4 * ga + 2 * (bt - 2) + fgi;
  const int mbase = min(40 * mj, 1496);

  const unsigned xo0 = (32 * w + (l >> 3)) * 1280 + 16 * ((l & 7) ^ ((l >> 3) & 7));
  const char* xs = uptr((const char*)x_ + (size_t)tg * 256 * 1280);
  const int wsub = tq >> 1;
  const int rs = kind == 3 ? 3072 : 3840;
  unsigned wo[2];
#pragma unroll
  for (int i = 0; i < 2; ++i) {
    const int c = min(64 * i + l, 79), pl = c / 5, ch = c - pl * 5;
    const int pos = 16 * (tq & 1) + pl;
    const int krow = (pos & 19) | ((pos & 4) << 1) | ((pos & 8) >> 1);
    wo[i] = krow * rs + 16 * ch;
  }
  const char* wsrc = uptr(kind == 3 ? (const char*)(wsub ? wu_ : wg_) + mbase * 2
                                    : (const char*)wqkv_ + (640 * kind + 80 * ga + 40 * wsub) * 2);
  const int winc = 32 * rs;
  const int xdst = lb + L_XR + w * 4096;
  const int wdst = lb + L_WR + fgi * WREG + wsub * SUB + (tq & 1) * 1280;
  auto issue_x = [&](auto J, auto O) {
    constexpr int j = decltype(J)::value, o = decltype(O)::value;
    if constexpr (j < 10) dma16s<j * 128>(xo0, xs + o * 10240, xdst + (j % NXS) * XSTG + o * 1024);
  };
  auto issue_w = [&](auto S, auto O) {
    constexpr int s = decltype(S)::value, o = decltype(O)::value;
    if constexpr (s < 20) {
      if constexpr (o == 0) dma16s(wo[0], wsrc, wdst + (s % NWS) * WSTG);
      if constexpr (o == 1) { dma16s_q(wo[1], wsrc, wdst + (s % NWS) * WSTG + 1024); wsrc += winc; }
    }
  };
  sfor<0, 4>([&](auto O) { issue_x(IC<0>{}, O); });
  issue_w(IC<0>{}, IC<0>{}); issue_w(IC<0>{}, IC<1>{});
  const int gw = b * 8 + w;
  dma4(warm_addr(gw * 11 + min(l, 10), 0, x_, wqkv_, wg_, wu_), lb + L_DUMMY);
  BAR();
  issue_w(IC<1>{}, IC<0>{}); issue_w(IC<1>{}, IC<1>{});
  dma4(warm_addr(gw * 11 + min(l, 10), 1, x_, wqkv_, wg_, wu_), lb + L_DUMMY);
  const unsigned tag = ((unsigned)llvm_dispatch_id() + 1u) * 0x9E3779B1u ^ 0x5BD1E995u;
  u32x4 xr0, xr1;
  {
    const char* xrow = uptr((const char*)x_ + (size_t)gw * 1280);
    gload16a(xr0, l * 16, xrow);
    gload16a(xr1, 1024 + (l & 15) * 16, xrow);
  }
  const int mb = (ga * 2 + (bt - 2)) * 8 + tg;
  const int mw = mb * 8 + w;
  auto side_dma = [&]() {
    if (bt >= 2) {
#pragma unroll
      for (int i = 0; i < 3; ++i) {
        const int fr = min(mw + 1024 * i, 2719);
        const int fg = fr / 340, rem = fr - fg * 340, ks = rem / 5, nt = rem - ks * 5;
        const int slot = l >> 1, g_ = slot >> 3, j = slot & 7;
        const int u = (ks - 48) / 5, s_ = (ks - 48) - u * 5;
        const int id = 8 * s_ + j, e = id / 5, v = id - e * 5;
        const int d = v < 4 ? 4 * g_ + v : 16 + g_;
        const int row = ks < 48 ? 32 * ks + slot : (8 * u + e) * 20 + d;
        const char* pb_ = uptr(ks < 48 ? (const char*)wd_ : (const char*)wo_);
        dma16s(row * 1280 + (80 * fg + 16 * nt + 8 * (l & 1)) * 2, pb_, lb + L_PT + (w * 3 + i) * 1024);
      }
    }
    dma16s(ga * 3200 + min((w & 3) * 64 + l, 199) * 16, (const char*)wphi_, lb + L_WPHI + (w & 3) * 1024);
  };

  int tok = l & 15, g = l >> 4, q = (l & 15) >> 2, p = l & 3;
  auto relane = [&]() { tid = opaque(threadIdx.x); l = tid & 63; tok = l & 15; g = l >> 4; q = (l & 15) >> 2; p = l & 3; };
  const int ab = opaque(L_WR + fgi * WREG + (p >> 1) * SUB + (16 * (g >> 1) + 4 * (g & 1) + q) * 80 + 40 * (p & 1));
  const int cb = opaque(ab + 8 * tq);
  int bb[2][2][2];
#pragma unroll
  for (int h = 0; h < 2; ++h)
#pragma unroll
    for (int e = 0; e < 2; ++e)
#pragma unroll
      for (int i = 0; i < 2; ++i)
        bb[h][e][i] = opaque(L_XR + (64 * tq + 32 * (i ^ fgi) + tok) * 128 + 16 * ((4 * e + g) ^ (tok & 7)) + h * 2 * XSTG);

  f32x4 acc[5][4];
#pragma unroll
  for (int t = 0; t < 5; ++t)
#pragma unroll
    for (int i = 0; i < 4; ++i) acc[t][i] = f32x4{0.f, 0.f, 0.f, 0.f};
  f32x4 csa = f32x4{0.f, 0.f, 0.f, 0.f}, csb = f32x4{0.f, 0.f, 0.f, 0.f};
  u32x4 ONES4 = u32x4{0x3F803F80u, 0x3F803F80u, 0x3F803F80u, 0x3F803F80u};
  asm volatile("" : "+v"(ONES4));
#pragma unroll
  for (int t = 0; t < 5; ++t)
#pragma unroll
    for (int i = 0; i < 4; ++i) pin4(acc[t][i]);
  pin4(csa); pin4(csb);
  const __amdgpu_buffer_rsrc_t rsw = __builtin_amdgcn_make_buffer_rsrc((void*)ws, 0, (int)WS_BYTES, 0x00020000);
  u32x4 stv[4], fc[2];
  auto ld_shared = [&]() {
#pragma unroll
    for (int i = 0; i < 4; ++i)
      stv[i] = __builtin_amdgcn_raw_buffer_load_b128(rsw, (tg * 256 + 64 * tq + 16 * ((i + 2 * fgi) & 3) + tok) * 16, (int)OFF_ST, 16);
  };
  auto sh_bad = [&]() -> bool {
    bool bad = false;
#pragma unroll
    for (int i = 0; i < 4; ++i) bad = bad || stv[i][1] != tag || stv[i][3] != tag;
    return __any(bad);
  };

  u32x4 fa[2][5], fb[2][4];
  auto readA = [&](auto S, auto T) {
    constexpr int s = decltype(S)::value, t = decltype(T)::value;
    fa[s & 1][t] = trread2(L + ab + (s % NWS) * WSTG + 8 * t, 640);
  };
  auto readB = [&](auto S, auto I) {
    constexpr int s = decltype(S)::value, i = decltype(I)::value;
    constexpr int sl = (s / 2) % NXS, h = sl >= 2 ? 1 : 0;
    fb[s & 1][i] = *(LAS u32x4*)(L + bb[h][s & 1][i >> 1] + (sl - 2 * h) * XSTG + (i & 1) * 2048);
  };

  sfor<0, 4>([&](auto O) { issue_x(IC<1>{}, O); });
  issue_w(IC<2>{}, IC<0>{}); issue_w(IC<2>{}, IC<1>{});
  dma4(warm_addr(gw * 11 + min(l, 10), 2, x_, wqkv_, wg_, wu_), lb + L_DUMMY);
  STAMP(0, 1);
  WAIT_V(pend_at(-1));
  BAR();
  STAMP(0, 2);
  sfor<0, 4>([&](auto I) { readB(IC<0>{}, I); });
  sfor<0, 5>([&](auto T) { readA(IC<0>{}, T); });
  fc[0] = trread2(L + cb, 640);
  if (tid < 64) *(LAS u32x4*)(L + L_ZERO + tid * 16) = u32x4{0, 0, 0, 0};
  SCHED();

  sfor<0, 20>([&](auto S) {
    constexpr int s = decltype(S)::value;
    if constexpr (s + 1 < 20) {
      constexpr int pend = pend_at(s);
      asm volatile("s_waitcnt vmcnt(%0) lgkmcnt(0)" ::"i"(pend) : "memory");
      BAR();
    }
    sfor<0, 5>([&](auto T) {
      constexpr int t = decltype(T)::value;
      sfor<0, 4>([&](auto I) {
        constexpr int i = decltype(I)::value;
        { constexpr int n = 4 * t + i, tt = n % 5, ii = n / 5; mfma16a(acc[tt][ii], fa[s & 1][tt], fb[s & 1][ii]); }
        if constexpr (s + 1 < 20) {
          if constexpr (t == 0) readB(IC<s + 1>{}, I);
          if constexpr (t >= 1 && (i & 1) == 0 && 2 * (t - 1) + (i >> 1) < 5) readA(IC<s + 1>{}, IC<2 * (t - 1) + (i >> 1)>{});
        }
        if constexpr (i == 3 && t < 2) issue_x(IC<s / 2 + 2>{}, IC<2 * (s & 1) + t>{});
        if constexpr (i == 3 && (t == 2 || t == 3)) issue_w(IC<s + 3>{}, IC<t - 2>{});
        if constexpr (t == 3 && i == 2 && s + 1 < 20) fc[(s + 1) & 1] = trread2(L + cb + ((s + 1) % NWS) * WSTG, 640);
        if constexpr (t == 2 && i == 1) mfma16a(csa, fc[s & 1], ONES4);
        SCHED();
      });
    });
    if ((s & 3) == tq) mfma16a(csb, fa[s & 1][4], ONES4);
    if constexpr (s == 1) {
      relane();
      pinu(xr0); pinu(xr1);
      float v[16];
#pragma unroll
      for (int j = 0; j < 4; ++j) {
        v[2 * j] = __uint_as_float(xr0[j] << 16); v[2 * j + 1] = __uint_as_float(xr0[j] & 0xffff0000u);
        v[8 + 2 * j] = __uint_as_float(xr1[j] << 16); v[9 + 2 * j] = __uint_as_float(xr1[j] & 0xffff0000u);
      }
      const bool ex = l < 16;
      float a = 0.f, c = 0.f;
#pragma unroll
      for (int j = 0; j < 8; ++j) { a += v[j]; c += v[8 + j]; }
      const float mean = wave_sum(a + (ex ? c : 0.f)) * (1.f / 640.f);
      a = 0.f; c = 0.f;
#pragma unroll
      for (int j = 0; j < 8; ++j) { const float d0 = v[j] - mean, d1 = v[8 + j] - mean; a = fmaf(d0, d0, a); c = fmaf(d1, d1, c); }
      const float var = wave_sum(a + (ex ? c : 0.f)) * (1.f / 640.f);
      gst(ws + OFF_ST + (size_t)gw * 16, u32x4{__float_as_uint(-mean), tag, __float_as_uint(var + 1e-5f), tag});
    }
    if constexpr (s == 0) { relane(); dma4(warm_addr2(gw * 11 + min(l, 10), wd_, wo_), lb + L_DUMMY); }
    if constexpr (s == S_SIDE) { relane(); side_dma(); ld_shared(); }
#define ST_A 4
#define ST_B 9
#define ST_C 14
    if constexpr (s == ST_A) STAMP(0, 3);
    if constexpr (s == ST_B) STAMP(0, 4);
    if constexpr (s == ST_C) STAMP(0, 5);
    SCHED();
  });
  asm volatile("s_nop 7\n\ts_nop 7" ::: "memory");
  SCHED();
  STAMP(0, 6);
  relane();
  WAIT_V(0);
  if (__builtin_expect(sh_bad(), 0)) {
    unsigned spin = 0;
    do {
      __builtin_amdgcn_s_sleep(4);
      ld_shared();
    } while (sh_bad() && ++spin < (1u << 16));
  }
  if (tok == 0) {
    *(LAS f32x4*)(L + L_CSS + (fgi * 128 + tq * 16 + 4 * g) * 4) = csa;
    *(LAS f32x4*)(L + L_CSS + (fgi * 128 + 64 + tq * 16 + 4 * g) * 4) = csb;
  }
  __syncthreads();
  float nmean[4], vare[4], rstd[4];
#pragma unroll
  for (int i = 0; i < 4; ++i) {
    nmean[i] = __uint_as_float(stv[i][0]); vare[i] = __uint_as_float(stv[i][2]);
    rstd[i] = __builtin_amdgcn_rsqf(vare[i]);
  }
#pragma unroll
  for (int t = 0; t < 5; ++t) {
    f32x4 c4 = *(LAS f32x4*)(L + L_CSS + (fgi * 128 + t * 16 + 4 * g) * 4);
    if (t == 4) {
#pragma unroll
      for (int k = 1; k < 4; ++k) c4 += *(LAS f32x4*)(L + L_CSS + (fgi * 128 + 64 + k * 16 + 4 * g) * 4);
    }
#pragma unroll
    for (int i = 0; i < 4; ++i)
#pragma unroll
      for (int c = 0; c < 4; ++c) acc[t][i][c] = fmaf(nmean[i], c4[c], acc[t][i][c]);
  }
  STAMP(0, 7);
  if (bt >= 2) {
#pragma unroll
    for (int i = 0; i < 3; ++i) {
      const u32x4 v = trread2(L + L_PT + (w * 3 + i) * 1024 + (8 * g + q) * 32 + 8 * p, 128);
      if (mw + 1024 * i < 2720) gst(ws + OFF_WT + (size_t)(mw + 1024 * i) * 1024 + l * 16, v);
    }
    const u32x4 sent = u32x4{0xFFFFFFFFu, 0xFFFFFFFFu, 0xFFFFFFFFu, 0xFFFFFFFFu};
    gst(ws + OFF_O + (size_t)mb * 20480 + tid * 16, sent);
    gst(ws + OFF_O + (size_t)mb * 20480 + 8192 + tid * 16, sent);
    if (tid < 256) gst(ws + OFF_O + (size_t)mb * 20480 + 16384 + tid * 16, sent);
    if (mb < 105 && tid < 32) gst(ws + OFF_KVT + mb * 512 + tid * 16, sent);
  }
  const int tile0 = tg * 16 + tq * 4;

  if (kind == 3) {
    const int hi = l >> 5, pp = g & 1;
#pragma unroll
    for (int i = 0; i < 2; ++i) {
      const float rs_ = hi ? rstd[i + 2] : rstd[i], ve = hi ? vare[i + 2] : vare[i];
      const float c1 = -2.0f * 0.7978845608028654f * 1.4426950408889634f;
      const float cA = c1 * rs_, cB = c1 * 0.044715f * rs_ * rs_ * rs_;
      unsigned hp[10];
#pragma unroll
      for (int t = 0; t < 5; ++t) {
#pragma unroll
        for (int c2 = 0; c2 < 2; ++c2) {
          f32x2 gg, uu;
#pragma unroll
          for (int c = 0; c < 2; ++c) {
            const float va = acc[t][i][2 * c2 + c], vb = acc[t][i + 2][2 * c2 + c];
            const auto sw = __builtin_amdgcn_permlane32_swap(__float_as_uint(va), __float_as_uint(vb), false, false);
            const unsigned s0 = sw[0], s1 = sw[1];
            gg[c] = __uint_as_float(s0); uu[c] = __uint_as_float(s1);
          }
          const f32x2 arg = __builtin_elementwise_fma(gg * gg, f32x2{cB, cB}, f32x2{cA, cA}) * gg;
          const f32x2 e = f32x2{__builtin_amdgcn_exp2f(arg[0]), __builtin_amdgcn_exp2f(arg[1])};
          const f32x2 dn = __builtin_elementwise_fma(e, f32x2{ve, ve}, f32x2{ve, ve});
          const f32x2 rr = f32x2{__builtin_amdgcn_rcpf(dn[0]), __builtin_amdgcn_rcpf(dn[1])};
          const f32x2 hv = gg * uu * rr;
          hp[2 * t + c2] = pk2(hv[0], hv[1]);
        }
      }
      const int tl = tile0 + ((i + 2 * hi + 2 * fgi) & 3);
      char* dst = ws + OFF_H + (size_t)tl * 49152 + (mbase >> 3) * 256 + tok * 16;
      if (pp == 0) {
        gst(dst, u32x4{hp[0], hp[1], hp[2], hp[3]});
        gst(dst + 256, u32x4{hp[4], hp[5], hp[6], hp[7]});
        gst(dst + 512, u32x2{hp[8], hp[9]});
      } else {
        gst(dst + 520, u32x2{hp[0], hp[1]});
        gst(dst + 768, u32x4{hp[2], hp[3], hp[4], hp[5]});
        gst(dst + 1024, u32x4{hp[6], hp[7], hp[8], hp[9]});
      }
    }
  } else {
    u32x4 bq[4][3];
    if (kind != 2) {
#pragma unroll
      for (int i = 0; i < 4; ++i) {
#pragma unroll
        for (int s = 0; s < 2; ++s)
          bq[i][s] = u32x4{pk2(acc[2 * s][i][0], acc[2 * s][i][1]), pk2(acc[2 * s][i][2], acc[2 * s][i][3]),
                           pk2(acc[2 * s + 1][i][0], acc[2 * s + 1][i][1]), pk2(acc[2 * s + 1][i][2], acc[2 * s + 1][i][3])};
        bq[i][2] = u32x4{pk2(acc[4][i][0], acc[4][i][1]), pk2(acc[4][i][2], acc[4][i][3]), 0u, 0u};
      }
    }
    if (bt == 0) {
      LAS char* xch = L + L_PK + (64 * tq + 32) * PK_PITCH + l * 16;
      if (kind == 1) {
#pragma unroll
        for (int j = 0; j < 6; ++j) *(LAS u32x4*)(xch + j * 1024) = bq[2 + j / 3][j % 3];
      } else {
#pragma unroll
        for (int i = 0; i < 4; ++i) {
          const int trow = 64 * tq + 16 * ((i + 2) & 3) + tok;
#pragma unroll
          for (int t = 0; t < 5; ++t)
            *(LAS u32x2*)(L + L_V + trow * V_PITCH + 40 * g + 8 * t) =
                u32x2{pk2(acc[t][i][0] * rstd[i], acc[t][i][1] * rstd[i]), pk2(acc[t][i][2] * rstd[i], acc[t][i][3] * rstd[i])};
          if (g == 0) *(LAS u32x2*)(L + L_V + trow * V_PITCH + 160) = u32x2{0x00003F80u, 0u};
        }
      }
      __syncthreads();
      if (kind == 2) {
#pragma unroll
        for (int j = 0; j < 6; ++j) bq[j / 3][j % 3] = *(LAS u32x4*)(xch + j * 1024);
      }
    }
    const int wb = (p == g) ? (L_WPHI + g * 800 + q * 40) : L_ZERO;
    auto phi2 = [&](auto I0) {
      constexpr int i0 = decltype(I0)::value;
      f32x4 pq[5][2];
#pragma unroll
      for (int tt = 0; tt < 5; ++tt) {
        const u32x4 a0 = trread2(L + wb + 8 * tt, 160), a1 = trread2(L + wb + 8 * tt + 320, 160);
        const u32x2 a2l = trread(L + wb + 8 * tt + 640);
        const u32x4 a2 = u32x4{a2l[0], a2l[1], 0u, 0u};
#pragma unroll
        for (int i = 0; i < 2; ++i) {
          f32x4 o = f32x4{0.f, 0.f, 0.f, 0.f};
          o = mfma16(a0, bq[i0 + i][0], o);
          o = mfma16(a1, bq[i0 + i][1], o);
          o = mfma16(a2, bq[i0 + i][2], o);
          pq[tt][i] = o;
        }
      }
#pragma unroll
      for (int i = 0; i < 2; ++i) {
        const float c1 = rstd[i0 + i] * 1.4426950408889634f;
        float mx = pq[0][i][0], mn = pq[0][i][0];
#pragma unroll
        for (int f = 1; f < 20; ++f) { mx = fmaxf(mx, pq[f >> 2][i][f & 3]); mn = fminf(mn, pq[f >> 2][i][f & 3]); }
        const float bx = -mx * c1, bn = mn * c1;
        f32x2 ep[10], en[10], sp2 = f32x2{0.f, 0.f}, sn2 = f32x2{0.f, 0.f};
#pragma unroll
        for (int f = 0; f < 10; ++f) {
          const f32x2 pv = f32x2{pq[f >> 1][i][2 * (f & 1)], pq[f >> 1][i][2 * (f & 1) + 1]};
          const f32x2 ap = __builtin_elementwise_fma(pv, f32x2{c1, c1}, f32x2{bx, bx});
          const f32x2 an = __builtin_elementwise_fma(pv, f32x2{-c1, -c1}, f32x2{bn, bn});
          ep[f] = f32x2{__builtin_amdgcn_exp2f(ap[0]), __builtin_amdgcn_exp2f(ap[1])};
          en[f] = f32x2{__builtin_amdgcn_exp2f(an[0]), __builtin_amdgcn_exp2f(an[1])};
          sp2 += ep[f]; sn2 += en[f];
        }
        const float rp = __builtin_amdgcn_rcpf(sp2[0] + sp2[1]), rn = __builtin_amdgcn_rcpf(sn2[0] + sn2[1]);
        unsigned o[20];
#pragma unroll
        for (int f = 0; f < 10; ++f) {
          const f32x2 a = ep[f] * f32x2{rp, rp}, c = en[f] * f32x2{rn, rn};
          o[f] = pk2(a[0], a[1]);
          o[10 + f] = pk2(c[0], c[1]);
        }
        if (kind == 0) {
          char* dst = ws + OFF_PQ + (size_t)((tile0 + i0 + i) * 8 + ga) * 5120 + tok * 16;
#pragma unroll
          for (int ci = 0; ci < 4; ++ci) gst(dst + g * 1024 + ci * 256, u32x4{o[4 * ci], o[4 * ci + 1], o[4 * ci + 2], o[4 * ci + 3]});
          gst(dst + 4096 + g * 256, u32x4{o[16], o[17], o[18], o[19]});
        } else {
          const int trow = 64 * tq + 32 * fgi + 16 * i + tok;
#pragma unroll
          for (int ci = 0; ci < 5; ++ci)
            *(LAS u32x4*)(L + L_PK + trow * PK_PITCH + g * 80 + ci * 16) = u32x4{o[4 * ci], o[4 * ci + 1], o[4 * ci + 2], o[4 * ci + 3]};
        }
      }
    };
    phi2(IC<0>{});
    if (kind == 0) phi2(IC<2>{});
  }
  if (bt == 0) {
    __syncthreads();
    const int e = w >> 1, bh = w & 1;
    const int rowoff = 8 * g + q;
    LAS char* pa = L + L_PK + rowoff * PK_PITCH + e * 80 + 8 * p;
    LAS char* pa2 = L + L_PK + rowoff * PK_PITCH + e * 80 + 64 + 8 * (p & 1);
    LAS char* pb = L + L_V + rowoff * V_PITCH + (bh == 0 ? e * 40 + 8 * p : (p == 0 ? e * 40 + 32 : 160));
    f32x4 d[3];
#pragma unroll
    for (int k = 0; k < 3; ++k) d[k] = f32x4{0.f, 0.f, 0.f, 0.f};
#pragma unroll
    for (int ks = 0; ks < 8; ++ks) {
      const u32x4 bb = trread2(pb + ks * 32 * V_PITCH, 4 * V_PITCH);
      d[0] = mfma16(trread2(pa + ks * 32 * PK_PITCH, 4 * PK_PITCH), bb, d[0]);
      d[1] = mfma16(trread2(pa + ks * 32 * PK_PITCH + 32, 4 * PK_PITCH), bb, d[1]);
      d[2] = mfma16(trread2(pa2 + ks * 32 * PK_PITCH, 4 * PK_PITCH), bb, d[2]);
    }
    const int dcol = 16 * bh + tok;
    if (dcol <= 20) {
      float* dst = (float*)(ws + OFF_KVP) + ((size_t)(tg * 32 + 4 * ga + e) * 21 + dcol) * 40 + 4 * g;
      *(f32x4*)(dst) = d[0];
      *(f32x4*)(dst + 16) = d[1];
      if (g < 2) *(f32x4*)(dst + 32) = d[2];
    }
  }
  asm volatile("" ::"v"(cpf));
  STAMP(0, 8);
  WAIT_V(0);
  STAMP_FLUSH(0);
}

constexpr int K2_LDS = 163840;
__global__ __launch_bounds__(512, 2) void k2_out(const __hip_bfloat16* __restrict__ x_, char* __restrict__ ws,
                                                 __hip_bfloat16* __restrict__ out_) {
  extern __shared__ __attribute__((aligned(16))) char smem[];
  LAS char* L = (LAS char*)smem;
  const int tid = threadIdx.x, l = tid & 63;
  const int w = __builtin_amdgcn_readfirstlane(tid >> 6);
  const int b = blockIdx.x, xcd = b & 7, idx = b >> 3;
  STAMP_DECL;
  STAMP(1, 0);
  const __amdgpu_buffer_rsrc_t rs = __builtin_amdgcn_make_buffer_rsrc((void*)ws, 0, (int)WS_BYTES, 0x00020000);
  const bool red = (w == 7 && b < 210);
  f32x2 pv[NP];
  if (w == 7) __builtin_amdgcn_s_setprio(3);
  if (red) {
    const GAS f32x2* pp = (const GAS f32x2*)(ws + OFF_KVP) + b * 64 + l;
#pragma unroll
    for (int k = 0; k < NP; ++k) pv[k] = pp[k * 13440];
  }
  SCHED();
  BAR();
  const unsigned cpf = code_prefetch<K2_PF>(idx, tid);
  const int tg = xcd * 4 + (idx & 3);
  const int fg = idx >> 2;
  const int vo = l * 16;
  const int so_h = (int)OFF_H + tg * 4 * 49152 + 6 * w * 1024;
  const int so_a = (int)OFF_WT + fg * 68 * 5120 + 6 * w * 5120;
  const int ok0 = w < 4 ? 2 * w : 3 * w - 4;
  const int so_o = (int)OFF_WT + fg * 68 * 5120 + (48 + ok0) * 5120;
  const int so_f = (int)OFF_O + (tg * 80 + ok0) * 1024;
  auto ldg = [&](int so) -> u32x4 { return __builtin_amdgcn_raw_buffer_load_b128(rs, vo, so, 0); };
  f32x4 acc[5][4];
#pragma unroll
  for (int nt = 0; nt < 5; ++nt)
#pragma unroll
    for (int i = 0; i < 4; ++i) { acc[nt][i] = f32x4{0.f, 0.f, 0.f, 0.f}; pin4(acc[nt][i]); }

  u32x4 fa[6][5], fb[6][4], ca[3][5], ob[3][4];
  auto ld = [&](auto K) {
    constexpr int k = decltype(K)::value;
#pragma unroll
    for (int i = 0; i < 4; ++i) fb[k][i] = ldg(so_h + i * 49152 + k * 1024);
#pragma unroll
    for (int nt = 0; nt < 5; ++nt) fa[k][nt] = ldg(so_a + k * 5120 + nt * 1024);
    SCHED();
  };
  auto mm = [&](auto K) {
    constexpr int k = decltype(K)::value;
#pragma unroll
    for (int nt = 0; nt < 5; ++nt)
#pragma unroll
      for (int i = 0; i < 4; ++i) mfma16a(acc[nt][i], fa[k][nt], fb[k][i]);
    SCHED();
  };
  auto ldo = [&](int s) {
#pragma unroll
    for (int nt = 0; nt < 5; ++nt) ca[s][nt] = ldg(so_o + s * 5120 + nt * 1024);
    SCHED();
  };
  auto ldf = [&](int s) {
#pragma unroll
    for (int i = 0; i < 4; ++i) ob[s][i] = __builtin_amdgcn_raw_buffer_load_b128(rs, vo, so_f + i * 20480 + s * 1024, 16);
  };
  auto fbad = [&](int s) -> bool {
    unsigned mxw = 0u;
#pragma unroll
    for (int i = 0; i < 4; ++i)
#pragma unroll
      for (int j = 0; j < 4; ++j) mxw = max(mxw, ob[s][i][j]);
    return __any(mxw == 0xFFFFFFFFu);
  };
  auto ostep = [&](int s) {
    if (__builtin_expect(fbad(s), 0)) {
      unsigned spin = 0;
      do {
        __builtin_amdgcn_s_sleep(4);
        ldf(s);
      } while (fbad(s) && ++spin < (1u << 14));
    }
#pragma unroll
    for (int nt = 0; nt < 5; ++nt)
#pragma unroll
      for (int i = 0; i < 4; ++i) mfma16a(acc[nt][i], ca[s][nt], ob[s][i]);
    SCHED();
  };

  if (red) {
    f32x2 s = pv[0];
#pragma unroll
    for (int k = 1; k < NP; ++k) s += pv[k];
    __builtin_amdgcn_raw_buffer_store_b32(pk2(s[0], s[1]), rs, (int)OFF_KVT + b * 256 + l * 4, 0, 16);
  }
  __builtin_amdgcn_s_setprio(3);
  SCHED();
  STAMP(1, 1);
  ld(IC<0>{}); ld(IC<1>{}); ld(IC<2>{});
  __builtin_amdgcn_s_setprio(2);
  mm(IC<0>{}); ld(IC<3>{});
  asm volatile("" ::"v"(cpf));
  mm(IC<1>{});
  if (w < 4) {
    const int r = l & 15, g = l >> 4;
    const int row1 = (r & 3) == 0 ? 16 + (r >> 2) : 20;
    const int so_k = (int)OFF_KVT + 4 * fg * 1680;
    const int ko0 = r * 80 + g * 16, ko1 = row1 * 80 + g * 16, kt0 = r * 80 + g * 1680 + 64, kt1 = row1 * 80 + g * 1680 + 64;
    u32x4 pf[5], ka[4][2], kt[2];
#pragma unroll
    for (int f = 0; f < 5; ++f) pf[f] = ldg((int)OFF_PQ + ((tg * 4 + w) * 8 + fg) * 5120 + f * 1024);
    for (unsigned spin = 0; spin < (1u << 14); ++spin) {
#pragma unroll
      for (int hd = 0; hd < 4; ++hd) {
        ka[hd][0] = __builtin_amdgcn_raw_buffer_load_b128(rs, ko0, so_k + hd * 1680, 16);
        ka[hd][1] = __builtin_amdgcn_raw_buffer_load_b128(rs, ko1, so_k + hd * 1680, 16);
      }
      kt[0] = __builtin_amdgcn_raw_buffer_load_b128(rs, kt0, so_k, 16);
      kt[1] = __builtin_amdgcn_raw_buffer_load_b128(rs, kt1, so_k, 16);
      unsigned mxw = 0u;
#pragma unroll
      for (int k = 0; k < 2; ++k)
#pragma unroll
        for (int j = 0; j < 4; ++j) {
          mxw = max(mxw, kt[k][j]);
#pragma unroll
          for (int hd = 0; hd < 4; ++hd) mxw = max(mxw, ka[hd][k][j]);
        }
      if (!__any(mxw == 0xFFFFFFFFu)) break;
      __builtin_amdgcn_s_sleep(4);
    }
    float ov[20];
#pragma unroll
    for (int hd = 0; hd < 4; ++hd) {
      const u32x4 pt = (g == hd) ? pf[4] : u32x4{0u, 0u, 0u, 0u};
      f32x4 o0 = f32x4{0.f, 0.f, 0.f, 0.f}, o1 = f32x4{0.f, 0.f, 0.f, 0.f};
      o0 = mfma16(ka[hd][0], pf[hd], o0);
      o1 = mfma16(ka[hd][1], pf[hd], o1);
      o0 = mfma16(kt[0], pt, o0);
      o1 = mfma16(kt[1], pt, o1);
      const float inv = __builtin_amdgcn_rcpf(o1[1] + 1e-6f);
#pragma unroll
      for (int v = 0; v < 4; ++v) ov[5 * hd + v] = o0[v] * inv;
      ov[5 * hd + 4] = o1[0] * inv;
    }
    unsigned op[10];
#pragma unroll
    for (int j = 0; j < 10; ++j) op[j] = pk2(ov[2 * j], ov[2 * j + 1]);
    const int so_d = (int)OFF_O + ((tg * 4 + w) * 20 + (fg >> 1) * 5) * 1024;
    if ((fg & 1) == 0) {
      __builtin_amdgcn_raw_buffer_store_b128(u32x4{op[0], op[1], op[2], op[3]}, rs, vo, so_d, 16);
      __builtin_amdgcn_raw_buffer_store_b128(u32x4{op[4], op[5], op[6], op[7]}, rs, vo, so_d + 1024, 16);
      __builtin_amdgcn_raw_buffer_store_b64(u32x2{op[8], op[9]}, rs, vo, so_d + 2048, 16);
    } else {
      __builtin_amdgcn_raw_buffer_store_b64(u32x2{op[0], op[1]}, rs, vo + 8, so_d + 2048, 16);
      __builtin_amdgcn_raw_buffer_store_b128(u32x4{op[2], op[3], op[4], op[5]}, rs, vo, so_d + 3072, 16);
      __builtin_amdgcn_raw_buffer_store_b128(u32x4{op[6], op[7], op[8], op[9]}, rs, vo, so_d + 4096, 16);
    }
  }
  SCHED();
  STAMP(1, 2);
  ld(IC<4>{});
  __builtin_amdgcn_s_setprio(1);
  mm(IC<2>{}); ld(IC<5>{});
  mm(IC<3>{}); ldo(0); ldo(1);
  __builtin_amdgcn_s_setprio(0);
  mm(IC<4>{}); ldf(0); ldf(1); SCHED();
  if (w >= 4) { ldo(2); ldf(2); }
  SCHED();
  u32x2 xv[3];
#pragma unroll
  for (int k = 0; k < 3; ++k) {
    const int t = min(w + 8 * k, 19), nt = t >> 2, i = t & 3;
    xv[k] = *(const u32x2*)((const char*)x_ + ((size_t)(tg * 64 + i * 16 + (l & 15)) * 640 + fg * 80 + nt * 16 + 4 * (l >> 4)) * 2);
  }
  SCHED();
  mm(IC<5>{});
  STAMP(1, 3);
  ostep(0);
  ostep(1);
  if (w >= 4) ostep(2);
  SCHED();
  asm volatile("s_nop 7\n\ts_nop 7" ::: "memory");
  SCHED();
  STAMP(1, 4);
#pragma unroll
  for (int nt = 0; nt < 5; ++nt)
#pragma unroll
    for (int i = 0; i < 4; ++i) *(LAS f32x4*)(L + ((w * 20 + nt * 4 + i) * 64 + l) * 16) = acc[nt][i];
  __syncthreads();
  STAMP(1, 5);
#pragma unroll
  for (int k = 0; k < 3; ++k) {
    const int t = w + 8 * k;
    if (t < 20) {
      f32x4 s = *(LAS f32x4*)(L + (t * 64 + l) * 16);
#pragma unroll
      for (int ww = 1; ww < 8; ++ww) s += *(LAS f32x4*)(L + ((ww * 20 + t) * 64 + l) * 16);
      const int nt = t >> 2, i = t & 3;
      const size_t off = ((size_t)(tg * 64 + i * 16 + (l & 15)) * 640 + fg * 80 + nt * 16 + 4 * (l >> 4)) * 2;
      const float x0 = __builtin_bit_cast(float, xv[k][0] << 16), x1 = __builtin_bit_cast(float, xv[k][0] & 0xffff0000u);
      const float x2 = __builtin_bit_cast(float, xv[k][1] << 16), x3 = __builtin_bit_cast(float, xv[k][1] & 0xffff0000u);
      *(u32x2*)((char*)out_ + off) = u32x2{pk2(s[0] + x0, s[1] + x1), pk2(s[2] + x2, s[3] + x3)};
    }
  }
  if (tid < 8) *(u32x4*)(ws + OFF_ST + (size_t)(b * 8 + tid) * 16) = u32x4{0u, 0u, 0u, 0u};
  STAMP(1, 6);
  STAMP_FLUSH(1);
}

extern "C" void kernel_launch(void* const* d_in, const int* in_sizes, int n_in, void* d_out, int out_size, void* d_ws,
                              size_t ws_size, hipStream_t stream) {
  const __hip_bfloat16* x = (const __hip_bfloat16*)d_in[0];
  const __hip_bfloat16* wqkv = (const __hip_bfloat16*)d_in[1];
  const __hip_bfloat16* wphi = (const __hip_bfloat16*)d_in[2];
  const __hip_bfloat16* wo = (const __hip_bfloat16*)d_in[3];
  const __hip_bfloat16* wg = (const __hip_bfloat16*)d_in[5];
  const __hip_bfloat16* wu = (const __hip_bfloat16*)d_in[6];
  const __hip_bfloat16* wd = (const __hip_bfloat16*)d_in[7];
  (void)hipFuncSetAttribute((const void*)k1_proj, hipFuncAttributeMaxDynamicSharedMemorySize, K1_LDS);
  (void)hipFuncSetAttribute((const void*)k2_out, hipFuncAttributeMaxDynamicSharedMemorySize, K2_LDS);
  k1_proj<<<256, 512, K1_LDS, stream>>>(x, wqkv, wphi, wo, wg, wu, wd, (char*)d_ws);
  k2_out<<<256, 512, K2_LDS, stream>>>(x, (char*)d_ws, (__hip_bfloat16*)d_out);
}
```
